```python
import torch

def _softmax(x, axis=-1):
    return torch.softmax(x, dim=axis)
def _gelu(x):
    return torch.nn.functional.gelu(x, approximate="tanh")
def _silu(x):
    return torch.nn.functional.silu(x)
def _sigmoid(x):
    return torch.sigmoid(x)
def _layernorm(x, axis=-1, eps=1e-5):
    m = x.mean(dim=axis, keepdim=True)
    v = x.var(dim=axis, keepdim=True, unbiased=False)
    return (x - m) / torch.sqrt(v + eps)
def _scatter_add(tgt, idx, src, axis=0):
    return tgt.clone().index_add_(axis, idx, src)
def _segment_sum(src, seg, n):
    out = torch.zeros((n,) + src.shape[1:], dtype=src.dtype)
    return out.index_add_(0, seg, src)
def _pad(x, pw):
    flat = tuple(v for pair in reversed(pw) for v in pair)
    return torch.nn.functional.pad(x, flat)

kernel_name = 'gated_mix__ssm_conv_s4d__dense_activation_silu__prompt'

def setup_inputs():

    _w = (lambda a: (lambda t: t.to(torch.bfloat16) if t.is_floating_point() else t)(torch.as_tensor(a)))
    def _A_setup():
        Bsz, L, D, N = 1, 2048, 640, 128
        return {
            "u": _w((lambda *s: torch.randn(*s, dtype=torch.bfloat16) * 0.05)(Bsz, L, D)),
            "log_A_real": _w((lambda *s: torch.randn(*s, dtype=torch.bfloat16) * 3.2)(D, N) * 0.5),
            "A_imag": _w((lambda *s: torch.randn(*s, dtype=torch.bfloat16) * 0.05)(D, N) * 3.14),
            "C_real": _w((lambda *s: torch.randn(*s, dtype=torch.bfloat16) * 0.1)(D, N) / N ** 0.5),
            "C_imag": _w((lambda *s: torch.randn(*s, dtype=torch.bfloat16) * 0.1)(D, N) / N ** 0.5),
            "log_dt": _w((lambda *s: torch.randn(*s, dtype=torch.bfloat16) * 0.05)(D) * 0.5 - 4.0),
            "D_skip": _w((lambda *s: torch.randn(*s, dtype=torch.bfloat16) * 0.05)(D)),
        }
    _A_in = _A_setup()

    def _B_setup():
        B, S, D, F = 1, 2048, 640, 1728
        return {
            "x": _w((lambda *s: torch.randn(*s, dtype=torch.bfloat16) * 0.05)(B, S, D)),
            "w1": _w((lambda *s: torch.randn(*s, dtype=torch.bfloat16) * 3.2)(D, F) * (D ** -0.5)),
            "w2": _w((lambda *s: torch.randn(*s, dtype=torch.bfloat16) * 0.1)(F, D) * (F ** -0.5)),
        }
    _B_in = _B_setup()

    return {
        "A_u": _A_in["u"],
        "A_log_A_real": _A_in["log_A_real"],
        "A_A_imag": _A_in["A_imag"],
        "A_C_real": _A_in["C_real"],
        "A_C_imag": _A_in["C_imag"],
        "A_log_dt": _A_in["log_dt"],
        "A_D_skip": _A_in["D_skip"],
        "B_x": _B_in["x"],
        "B_w1": _B_in["w1"],
        "B_w2": _B_in["w2"],
        "gate_w": _w((lambda *s: torch.randn(*s, dtype=torch.bfloat16) * 0.05)(640) * 16.0),
    }

def reference(A_u, A_log_A_real, A_A_imag, A_C_real, A_C_imag, A_log_dt, A_D_skip, B_x, B_w1, B_w2, gate_w):
    def _A_ref(u, log_A_real, A_imag, C_real, C_imag, log_dt, D_skip):
        L = u.shape[1]
        dt = torch.exp(log_dt)
        A = -torch.exp(log_A_real) + 1j * A_imag
        C = (C_real + 1j * C_imag) * dt[:, None]
        pos = (lambda x, dt: x.to(dt))(torch.arange(L), log_dt.dtype)
        P = torch.exp((A * dt[:, None])[:, :, None] * pos[None, None, :])
        K = 2.0 * torch.einsum("dn,dnl->dl", C, P).real
        L2 = 2 * L
        u_t = (lambda x, *d: x.permute(*d))(u, 0, 2, 1)
        Kf = (lambda x, *a, **k: torch.fft.rfft(x.float(), *a, **k))(K, L2)
        Uf = (lambda x, *a, **k: torch.fft.rfft(x.float(), *a, **k))(u_t, L2)
        y = (lambda x, *a, **k: torch.fft.irfft(x, *a, **k).to(torch.bfloat16))(Uf * Kf[None, :, :], L2)[:, :, :L]
        y = (lambda x, *d: x.permute(*d))(y, 0, 2, 1)
        return y + u * D_skip[None, None, :]

    def _B_ref(x, w1, w2):
        h = torch.einsum("bsd,df->bsf", x, w1)
        h = _silu(h)
        out = torch.einsum("bsf,fd->bsd", h, w2)
        return out

    x = A_u
    g = _sigmoid((x * gate_w).sum(-1, keepdims=True))
    a = _A_ref(x, A_log_A_real, A_A_imag, A_C_real, A_C_imag, A_log_dt, A_D_skip)
    b = _B_ref(x, B_w1, B_w2)
    return g * a + (1 - g) * b
```

```cpp
#include <hip/hip_runtime.h>
#include <hip/hip_bf16.h>
#include <stdint.h>

#define L_SEQ 2048
#define D_CH 640
#define N_ST 128
#define F_FF 1728

typedef unsigned short u16;
typedef __attribute__((ext_vector_type(8))) short bf16x8;
typedef __attribute__((ext_vector_type(4))) short s16x4;
typedef __attribute__((ext_vector_type(4))) float f32x4;
typedef __attribute__((ext_vector_type(4))) unsigned u32x4;

__device__ __forceinline__ float bf2f(u16 v) { return __uint_as_float(((unsigned)v) << 16); }
__device__ __forceinline__ u16 f2bf(float f) {
  unsigned u = __float_as_uint(f);
  u += 0x7FFFu + ((u >> 16) & 1u);
  return (u16)(u >> 16);
}
__device__ __forceinline__ float rbf(float f) { return bf2f(f2bf(f)); }

__device__ __forceinline__ s16x4 lds_tr16(const u16* p) {
  return __builtin_amdgcn_ds_read_tr16_b64_v4i16((__attribute__((address_space(3))) s16x4*)(p));
}

#define G1_BM 128
#define G1_BN 96
#define G1_BK 64
#define G1_LDA 72
#define G1_LDB 104
#define G1_TILES_N (F_FF / G1_BN)
#define G1_TILES_M (L_SEQ / G1_BM)
#define G1_BLOCKS (G1_TILES_M * G1_TILES_N)
#define CONV_BLOCKS D_CH
#define GATE_BLOCKS 32

#define CV_US_ELEMS (256 + L_SEQ)
#define CV_R_ELEMS 2080
#define CV_TAB_N 4
#define CV_TAB_HI 132
#define CV_TAB_LO 32
#define CV_KP 656

struct __align__(16) ConvLds {
  u16 us[CV_US_ELEMS];
  float kp[4][CV_KP];
  float4 prm[N_ST];
  union {
    struct {
      float2 chi[4][CV_TAB_N][CV_TAB_HI];
      float2 zlo[4][CV_TAB_N][CV_TAB_LO];
    } t;
    u16 r[4][CV_R_ELEMS];
  } un;
};

struct __align__(16) G1Lds {
  u16 as[G1_BM * G1_LDA];
  u16 bs[G1_BK * G1_LDB];
};

__device__ __forceinline__ int pos_of_idx(int idx) {
  if (idx < 256) return idx;
  if (idx < 384) return 256 + 2 * (idx - 256);
  if (idx < 512) return 512 + 4 * (idx - 384);
  return 1024 + 8 * (idx - 512);
}

__device__ __forceinline__ int idx_of_pos(int l) {
  if (l < 256) return l;
  if (l < 512) {
    int m = (l + 1) >> 1;
    if (l & 1) { int lo = (l - 1) >> 1; int hi = lo + 1; m = (lo & 1) ? hi : lo; }
    else m = l >> 1;
    return 256 + (m - 128);
  }
  if (l < 1024) {
    int r = l & 3, b = l >> 2;
    int m = (r < 2) ? b : (r > 2 ? b + 1 : ((b & 1) ? b + 1 : b));
    return 384 + (m - 128);
  }
  {
    int r = l & 7, b = l >> 3;
    int m = (r < 4) ? b : (r > 4 ? b + 1 : ((b & 1) ? b + 1 : b));
    return 512 + (m - 128);
  }
}

__device__ void gemm1_block(int bid, const u16* __restrict__ x, const u16* __restrict__ w1,
                            u16* __restrict__ h, char* smem) {
  G1Lds& S = *reinterpret_cast<G1Lds*>(smem);
  const int tid = threadIdx.x;
  const int lane = tid & 63;
  const int wave = tid >> 6;
  const int tm = bid / G1_TILES_N, tn = bid % G1_TILES_N;
  const int m0 = tm * G1_BM, n0 = tn * G1_BN;
  const int wm = wave & 1, wn = wave >> 1;

  f32x4 acc[4][3];
#pragma unroll
  for (int i = 0; i < 4; i++)
#pragma unroll
    for (int j = 0; j < 3; j++) acc[i][j] = (f32x4){0.f, 0.f, 0.f, 0.f};

  u32x4 ra[4], rb[3];
#define G1_GLOAD(k0)                                                                         \
  {                                                                                          \
    _Pragma("unroll") for (int i = 0; i < 4; i++) {                                          \
      int c = tid + 256 * i;                                                                 \
      int r = c >> 3, cc = c & 7;                                                            \
      ra[i] = *reinterpret_cast<const u32x4*>(x + (size_t)(m0 + r) * D_CH + (k0) + cc * 8);  \
    }                                                                                        \
    _Pragma("unroll") for (int i = 0; i < 3; i++) {                                          \
      int c = tid + 256 * i;                                                                 \
      int r = c / 12, cc = c % 12;                                                           \
      rb[i] = *reinterpret_cast<const u32x4*>(w1 + (size_t)((k0) + r) * F_FF + n0 + cc * 8); \
    }                                                                                        \
  }
#define G1_LSTORE()                                                  \
  {                                                                  \
    _Pragma("unroll") for (int i = 0; i < 4; i++) {                  \
      int c = tid + 256 * i;                                         \
      int r = c >> 3, cc = c & 7;                                    \
      *reinterpret_cast<u32x4*>(&S.as[r * G1_LDA + cc * 8]) = ra[i]; \
    }                                                                \
    _Pragma("unroll") for (int i = 0; i < 3; i++) {                  \
      int c = tid + 256 * i;                                         \
      int r = c / 12, cc = c % 12;                                   \
      *reinterpret_cast<u32x4*>(&S.bs[r * G1_LDB + cc * 8]) = rb[i]; \
    }                                                                \
  }

  G1_GLOAD(0);
  const int NK = D_CH / G1_BK;
  for (int kt = 0; kt < NK; kt++) {
    __syncthreads();
    G1_LSTORE();
    __syncthreads();
    if (kt + 1 < NK) G1_GLOAD((kt + 1) * G1_BK);
#pragma unroll
    for (int ks = 0; ks < 2; ks++) {
      bf16x8 af[4], bfr[3];
#pragma unroll
      for (int i = 0; i < 4; i++) {
        int row = wm * 64 + i * 16 + (lane & 15);
        af[i] = *reinterpret_cast<const bf16x8*>(&S.as[row * G1_LDA + ks * 32 + 8 * (lane >> 4)]);
      }
#pragma unroll
      for (int j = 0; j < 3; j++) {
        int q = lane >> 4, ii = lane & 15;
        const u16* p0 = &S.bs[(ks * 32 + 8 * q + (ii >> 2)) * G1_LDB + wn * 48 + j * 16 + 4 * (ii & 3)];
        s16x4 lo = lds_tr16(p0);
        s16x4 hi = lds_tr16(p0 + 4 * G1_LDB);
        bfr[j] = (bf16x8){lo[0], lo[1], lo[2], lo[3], hi[0], hi[1], hi[2], hi[3]};
      }
#pragma unroll
      for (int i = 0; i < 4; i++)
#pragma unroll
        for (int j = 0; j < 3; j++)
          acc[i][j] = __builtin_amdgcn_mfma_f32_16x16x32_bf16(af[i], bfr[j], acc[i][j], 0, 0, 0);
    }
  }
#pragma unroll
  for (int i = 0; i < 4; i++)
#pragma unroll
    for (int j = 0; j < 3; j++) {
      int col = n0 + wn * 48 + j * 16 + (lane & 15);
#pragma unroll
      for (int r = 0; r < 4; r++) {
        int row = m0 + wm * 64 + i * 16 + 4 * (lane >> 4) + r;
        float v = rbf(acc[i][j][r]);
        float s = v / (1.f + __expf(-v));
        h[(size_t)row * F_FF + col] = f2bf(s);
      }
    }
}

__device__ void conv_block(int d, const u16* __restrict__ u, const u16* __restrict__ logAre,
                           const u16* __restrict__ Aim, const u16* __restrict__ Cre,
                           const u16* __restrict__ Cim, const u16* __restrict__ logdt,
                           const u16* __restrict__ Dsk, u16* __restrict__ aout, char* smem) {
  ConvLds& S = *reinterpret_cast<ConvLds*>(smem);
  const int tid = threadIdx.x;
  const int lane = tid & 63;
  const int wave = tid >> 6;

  for (int i = tid; i < 256; i += 256) S.us[i] = 0;
#pragma unroll
  for (int i = 0; i < 8; i++) {
    int l = tid + 256 * i;
    S.us[256 + l] = u[(size_t)l * D_CH + d];
  }
  const float dt = rbf(expf(bf2f(logdt[d])));
  if (tid < N_ST) {
    int n = tid;
    float are = -rbf(expf(bf2f(logAre[d * N_ST + n])));
    float aim = bf2f(Aim[d * N_ST + n]);
    float cre = bf2f(Cre[d * N_ST + n]) * dt;
    float cim = bf2f(Cim[d * N_ST + n]) * dt;
    S.prm[n] = make_float4(are * dt, aim * dt, 2.f * cre, 2.f * cim);
  }
  __syncthreads();

  int hi0, his, lo0, los, obase;
  {
    if (lane < 16) { hi0 = 4 * (lane >> 2); his = 1; lo0 = 4 * (lane & 3); los = 1; }
    else if (lane < 24) { int t = lane - 16; hi0 = 16 + 4 * (t >> 1); his = 1; lo0 = 8 * (t & 1); los = 2; }
    else if (lane < 32) { int t = lane - 24; hi0 = 32 + 4 * t; his = 1; lo0 = 0; los = 4; }
    else if (lane < 40) { int t = lane - 32; hi0 = 64 + 8 * t; his = 2; lo0 = 0; los = 8; }
    else { hi0 = 128; his = 0; lo0 = 0; los = 0; }
    obase = 0;
  }
  float outp[4][4];
#pragma unroll
  for (int a = 0; a < 4; a++)
#pragma unroll
    for (int b = 0; b < 4; b++) outp[a][b] = 0.f;

  for (int chunk = 0; chunk < 32 / CV_TAB_N; chunk++) {
    for (int it = lane; it < CV_TAB_N * (129 + CV_TAB_LO); it += 64) {
      int nl = it / (129 + CV_TAB_LO);
      int rem = it - nl * (129 + CV_TAB_LO);
      int n = 32 * wave + chunk * CV_TAB_N + nl;
      float4 pr = S.prm[n];
      if (rem < 129) {
        float p = (float)(16 * rem);
        float e = expf(pr.x * p);
        float sn, cs;
        sincosf(pr.y * p, &sn, &cs);
        float er = e * cs, ei = e * sn;
        S.un.t.chi[wave][nl][rem] = make_float2(pr.z * er - pr.w * ei, pr.z * ei + pr.w * er);
      } else {
        int lo = rem - 129;
        float p = (float)lo;
        float e = expf(pr.x * p);
        float sn, cs;
        sincosf(pr.y * p, &sn, &cs);
        S.un.t.zlo[wave][nl][lo] = make_float2(e * cs, e * sn);
      }
    }
    __builtin_amdgcn_wave_barrier();
    __builtin_amdgcn_s_waitcnt(0xc07f);
#pragma unroll
    for (int nl = 0; nl < CV_TAB_N; nl++) {
      float2 ch[4], zl[4];
#pragma unroll
      for (int a = 0; a < 4; a++) ch[a] = S.un.t.chi[wave][nl][hi0 + a * his];
#pragma unroll
      for (int b = 0; b < 4; b++) zl[b] = S.un.t.zlo[wave][nl][lo0 + b * los];
#pragma unroll
      for (int a = 0; a < 4; a++)
#pragma unroll
        for (int b = 0; b < 4; b++)
          outp[a][b] = fmaf(ch[a].x, zl[b].x, fmaf(-ch[a].y, zl[b].y, outp[a][b]));
    }
    __builtin_amdgcn_wave_barrier();
  }
  if (lane < 41) {
#pragma unroll
    for (int a = 0; a < 4; a++)
#pragma unroll
      for (int b = 0; b < 4; b++) {
        int p = 16 * (hi0 + a * his) + lo0 + b * los;
        int idx;
        if (p < 256) idx = p;
        else if (p < 512) idx = 256 + ((p - 256) >> 1);
        else if (p < 1024) idx = 384 + ((p - 512) >> 2);
        else idx = 512 + ((p - 1024) >> 3);
        if (lane < 40 || (a == 0 && b == 0)) S.kp[wave][idx] = outp[a][b];
      }
  }
  __syncthreads();
  for (int x = tid; x < CV_R_ELEMS; x += 256) {
    int m = 2047 - x;
    float kv = 0.f;
    if (m >= 0) {
      int idx = idx_of_pos(m);
      kv = S.kp[0][idx] + S.kp[1][idx] + S.kp[2][idx] + S.kp[3][idx];
    }
    u16 hi = f2bf(kv);
    u16 lo = f2bf(kv - bf2f(hi));
    S.un.r[0][x] = hi;
    S.un.r[2][x] = lo;
    if (x + 1 < CV_R_ELEMS) {
      S.un.r[1][x + 1] = hi;
      S.un.r[3][x + 1] = lo;
    }
  }
  __syncthreads();

  const int r16 = lane & 15, q = lane >> 4;
  const int tA = wave, tB = 7 - wave;
  f32x4 accA = {0.f, 0.f, 0.f, 0.f}, accB = {0.f, 0.f, 0.f, 0.f};
  const int kmax = 16 * tB + 15;
  for (int k = 0; k <= kmax; k += 2) {
    int base = 16 * (k + 1) + r16 - 8 * q;
    int xx = 2047 - base;
    const unsigned* rh;
    const unsigned* rl;
    if (xx & 1) {
      rh = reinterpret_cast<const unsigned*>(&S.un.r[1][xx + 1]);
      rl = reinterpret_cast<const unsigned*>(&S.un.r[3][xx + 1]);
    } else {
      rh = reinterpret_cast<const unsigned*>(&S.un.r[0][xx]);
      rl = reinterpret_cast<const unsigned*>(&S.un.r[2][xx]);
    }
    union { unsigned w[4]; bf16x8 v; } ah, al;
#pragma unroll
    for (int i = 0; i < 4; i++) { ah.w[i] = rh[i]; al.w[i] = rl[i]; }
    {
      int s = 16 * tB - k;
      const u16* bp = &S.us[256 + 16 * (s + r16 - 1) + 8 * q];
      bf16x8 bfr = *reinterpret_cast<const bf16x8*>(bp);
      accB = __builtin_amdgcn_mfma_f32_16x16x32_bf16(ah.v, bfr, accB, 0, 0, 0);
      accB = __builtin_amdgcn_mfma_f32_16x16x32_bf16(al.v, bfr, accB, 0, 0, 0);
    }
    if (k <= 16 * tA + 15) {
      int s = 16 * tA - k;
      const u16* bp = &S.us[256 + 16 * (s + r16 - 1) + 8 * q];
      bf16x8 bfr = *reinterpret_cast<const bf16x8*>(bp);
      accA = __builtin_amdgcn_mfma_f32_16x16x32_bf16(ah.v, bfr, accA, 0, 0, 0);
      accA = __builtin_amdgcn_mfma_f32_16x16x32_bf16(al.v, bfr, accA, 0, 0, 0);
    }
  }
  const float dsk = bf2f(Dsk[d]);
#pragma unroll
  for (int r = 0; r < 4; r++) {
    {
      int l = 16 * (16 * tA + r16) + 4 * q + r;
      float uv = bf2f(S.us[256 + l]);
      float av = rbf(accA[r]) + rbf(uv * dsk);
      aout[(size_t)l * D_CH + d] = f2bf(av);
    }
    {
      int l = 16 * (16 * tB + r16) + 4 * q + r;
      float uv = bf2f(S.us[256 + l]);
      float av = rbf(accB[r]) + rbf(uv * dsk);
      aout[(size_t)l * D_CH + d] = f2bf(av);
    }
  }
}

__device__ void gate_block(int bid, const u16* __restrict__ x, const u16* __restrict__ gw,
                           float* __restrict__ g) {
  const int lane = threadIdx.x & 63;
  const int wave = threadIdx.x >> 6;
  float gwv[10];
#pragma unroll
  for (int i = 0; i < 10; i++) gwv[i] = bf2f(gw[lane + 64 * i]);
  for (int rr = 0; rr < 16; rr++) {
    int row = bid * 64 + wave * 16 + rr;
    float s = 0.f;
#pragma unroll
    for (int i = 0; i < 10; i++) s += rbf(bf2f(x[(size_t)row * D_CH + lane + 64 * i]) * gwv[i]);
#pragma unroll
    for (int off = 32; off > 0; off >>= 1) s += __shfl_xor(s, off);
    if (lane == 0) {
      float sv = rbf(s);
      g[row] = rbf(1.f / (1.f + __expf(-sv)));
    }
  }
}

__global__ void __launch_bounds__(256) k1(const u16* __restrict__ x, const u16* __restrict__ logAre,
                                          const u16* __restrict__ Aim, const u16* __restrict__ Cre,
                                          const u16* __restrict__ Cim, const u16* __restrict__ logdt,
                                          const u16* __restrict__ Dsk, const u16* __restrict__ w1,
                                          const u16* __restrict__ gw, u16* __restrict__ h,
                                          u16* __restrict__ aout, float* __restrict__ g) {
  __shared__ __align__(16) char smem[sizeof(ConvLds) > sizeof(G1Lds) ? sizeof(ConvLds) : sizeof(G1Lds)];
  int bid = blockIdx.x;
  if (bid < CONV_BLOCKS) {
    conv_block(bid, x, logAre, Aim, Cre, Cim, logdt, Dsk, aout, smem);
  } else if (bid < CONV_BLOCKS + G1_BLOCKS) {
    gemm1_block(bid - CONV_BLOCKS, x, w1, h, smem);
  } else {
    gate_block(bid - CONV_BLOCKS - G1_BLOCKS, x, gw, g);
  }
}

#define G2_BM 64
#define G2_BN 80
#define G2_BK 64
#define G2_LDA 72
#define G2_LDB 88
#define G2_TILES_N (D_CH / G2_BN)
#define G2_TILES_M (L_SEQ / G2_BM)

struct __align__(16) G2Lds {
  union {
    struct {
      u16 as[G2_BM * G2_LDA];
      u16 bs[G2_BK * G2_LDB];
    } t;
    float red[2][10][64][4];
  } un;
};

__global__ void __launch_bounds__(256) k2(const u16* __restrict__ h, const u16* __restrict__ w2,
                                          const u16* __restrict__ aout, const float* __restrict__ g,
                                          u16* __restrict__ out) {
  __shared__ G2Lds S;
  const int tid = threadIdx.x;
  const int lane = tid & 63;
  const int wave = tid >> 6;
  const int bid = blockIdx.x;
  const int tm = bid / G2_TILES_N, tn = bid % G2_TILES_N;
  const int m0 = tm * G2_BM, n0 = tn * G2_BN;
  const int wm = wave & 1, wk = wave >> 1;

  f32x4 acc[2][5];
#pragma unroll
  for (int i = 0; i < 2; i++)
#pragma unroll
    for (int j = 0; j < 5; j++) acc[i][j] = (f32x4){0.f, 0.f, 0.f, 0.f};

  u32x4 ra[2], rb[3];
#define G2_GLOAD(k0)                                                                          \
  {                                                                                           \
    _Pragma("unroll") for (int i = 0; i < 2; i++) {                                           \
      int c = tid + 256 * i;                                                                  \
      int r = c >> 3, cc = c & 7;                                                             \
      ra[i] = *reinterpret_cast<const u32x4*>(h + (size_t)(m0 + r) * F_FF + (k0) + cc * 8);   \
    }                                                                                         \
    _Pragma("unroll") for (int i = 0; i < 3; i++) {                                           \
      int c = tid + 256 * i;                                                                  \
      if (c < 640) {                                                                          \
        int r = c / 10, cc = c % 10;                                                          \
        rb[i] = *reinterpret_cast<const u32x4*>(w2 + (size_t)((k0) + r) * D_CH + n0 + cc * 8); \
      }                                                                                       \
    }                                                                                         \
  }
#define G2_LSTORE()                                                       \
  {                                                                       \
    _Pragma("unroll") for (int i = 0; i < 2; i++) {                       \
      int c = tid + 256 * i;                                              \
      int r = c >> 3, cc = c & 7;                                         \
      *reinterpret_cast<u32x4*>(&S.un.t.as[r * G2_LDA + cc * 8]) = ra[i]; \
    }                                                                     \
    _Pragma("unroll") for (int i = 0; i < 3; i++) {                       \
      int c = tid + 256 * i;                                              \
      if (c < 640) {                                                      \
        int r = c / 10, cc = c % 10;                                      \
        *reinterpret_cast<u32x4*>(&S.un.t.bs[r * G2_LDB + cc * 8]) = rb[i]; \
      }                                                                   \
    }                                                                     \
  }

  G2_GLOAD(0);
  const int NK = F_FF / G2_BK;
  for (int kt = 0; kt < NK; kt++) {
    __syncthreads();
    G2_LSTORE();
    __syncthreads();
    if (kt + 1 < NK) G2_GLOAD((kt + 1) * G2_BK);
    {
      const int ks = wk;
      bf16x8 af[2], bfr[5];
#pragma unroll
      for (int i = 0; i < 2; i++) {
        int row = wm * 32 + i * 16 + (lane & 15);
        af[i] = *reinterpret_cast<const bf16x8*>(&S.un.t.as[row * G2_LDA + ks * 32 + 8 * (lane >> 4)]);
      }
#pragma unroll
      for (int j = 0; j < 5; j++) {
        int q = lane >> 4, ii = lane & 15;
        const u16* p0 = &S.un.t.bs[(ks * 32 + 8 * q + (ii >> 2)) * G2_LDB + j * 16 + 4 * (ii & 3)];
        s16x4 lo = lds_tr16(p0);
        s16x4 hi = lds_tr16(p0 + 4 * G2_LDB);
        bfr[j] = (bf16x8){lo[0], lo[1], lo[2], lo[3], hi[0], hi[1], hi[2], hi[3]};
      }
#pragma unroll
      for (int i = 0; i < 2; i++)
#pragma unroll
        for (int j = 0; j < 5; j++)
          acc[i][j] = __builtin_amdgcn_mfma_f32_16x16x32_bf16(af[i], bfr[j], acc[i][j], 0, 0, 0);
    }
  }
  __syncthreads();
  if (wk == 1) {
#pragma unroll
    for (int i = 0; i < 2; i++)
#pragma unroll
      for (int j = 0; j < 5; j++)
        *reinterpret_cast<f32x4*>(&S.un.red[wm][i * 5 + j][lane][0]) = acc[i][j];
  }
  __syncthreads();
  if (wk == 0) {
#pragma unroll
    for (int i = 0; i < 2; i++)
#pragma unroll
      for (int j = 0; j < 5; j++) {
        f32x4 o = *reinterpret_cast<const f32x4*>(&S.un.red[wm][i * 5 + j][lane][0]);
        int col = n0 + j * 16 + (lane & 15);
#pragma unroll
        for (int r = 0; r < 4; r++) {
          int row = m0 + wm * 32 + i * 16 + 4 * (lane >> 4) + r;
          float b = rbf(acc[i][j][r] + o[r]);
          float gv = g[row];
          float av = bf2f(aout[(size_t)row * D_CH + col]);
          float t1 = rbf(gv * av);
          float t2 = rbf(rbf(1.f - gv) * b);
          out[(size_t)row * D_CH + col] = f2bf(t1 + t2);
        }
      }
  }
}

extern "C" void kernel_launch(void* const* d_in, const int* in_sizes, int n_in,
                              void* d_out, int out_size, void* d_ws, size_t ws_size,
                              hipStream_t stream) {
  const u16* u = (const u16*)d_in[0];
  const u16* logAre = (const u16*)d_in[1];
  const u16* Aim = (const u16*)d_in[2];
  const u16* Cre = (const u16*)d_in[3];
  const u16* Cim = (const u16*)d_in[4];
  const u16* logdt = (const u16*)d_in[5];
  const u16* Dsk = (const u16*)d_in[6];
  const u16* w1 = (const u16*)d_in[8];
  const u16* w2 = (const u16*)d_in[9];
  const u16* gw = (const u16*)d_in[10];
  u16* out = (u16*)d_out;
  char* ws = (char*)d_ws;
  u16* h = (u16*)ws;
  u16* aout = (u16*)(ws + (8u << 20));
  float* g = (float*)(ws + (12u << 20));

  k1<<<CONV_BLOCKS + G1_BLOCKS + GATE_BLOCKS, 256, 0, stream>>>(u, logAre, Aim, Cre, Cim, logdt, Dsk,
                                                                w1, gw, h, aout, g);
  k2<<<G2_TILES_M * G2_TILES_N, 256, 0, stream>>>(h, w2, aout, g, out);
}
```

```cpp
#include <hip/hip_runtime.h>
#include <hip/hip_bf16.h>
#include <stdint.h>

#define L_SEQ 2048
#define D_CH 640
#define N_ST 128
#define F_FF 1728

typedef unsigned short u16;
typedef __attribute__((ext_vector_type(8))) short bf16x8;
typedef __attribute__((ext_vector_type(4))) short s16x4;
typedef __attribute__((ext_vector_type(4))) float f32x4;
typedef __attribute__((ext_vector_type(4))) unsigned u32x4;

__device__ __forceinline__ float bf2f(u16 v) { return __uint_as_float(((unsigned)v) << 16); }
__device__ __forceinline__ u16 f2bf(float f) {
  unsigned u = __float_as_uint(f);
  u += 0x7FFFu + ((u >> 16) & 1u);
  return (u16)(u >> 16);
}
__device__ __forceinline__ float rbf(float f) { return bf2f(f2bf(f)); }

__device__ __forceinline__ s16x4 lds_tr16(const u16* p) {
  return __builtin_amdgcn_ds_read_tr16_b64_v4i16((__attribute__((address_space(3))) s16x4*)(p));
}

#define G1_BM 128
#define G1_BN 96
#define G1_BK 32
#define G1_LDA 40
#define G1_LDB 104
#define G1_TILES_N (F_FF / G1_BN)
#define G1_TILES_M (L_SEQ / G1_BM)
#define G1_BLOCKS (G1_TILES_M * G1_TILES_N)
#define CONV_BLOCKS D_CH
#define GATE_BLOCKS 128

#define CV_US_ELEMS (256 + L_SEQ)
#define CV_R_ELEMS 2080

struct __align__(16) ConvLds {
  u16 us[CV_US_ELEMS];
  float kp[4][768];
  float prm[N_ST][8];
  u16 r[4][CV_R_ELEMS];
};

#define G1_STAGE_BYTES (8192 + 6144)
struct __align__(16) G1Lds {
  u16 st[3][G1_STAGE_BYTES / 2];
  u16 dummy[512];
};

__device__ void gemm1_block(int bid, const u16* __restrict__ x, const u16* __restrict__ w1,
                            u16* __restrict__ h, char* smem) {
  G1Lds& S = *reinterpret_cast<G1Lds*>(smem);
  const int tid = threadIdx.x;
  const int lane = tid & 63;
  const int wave = tid >> 6;
  const int xcd = bid & 7, loc = bid >> 3;
  const int tm = 2 * xcd + loc / G1_TILES_N, tn = loc % G1_TILES_N;
  const int m0 = tm * G1_BM, n0 = tn * G1_BN;
  const int wm = wave & 1, wn = wave >> 1;

  f32x4 acc[4][3];
#pragma unroll
  for (int i = 0; i < 4; i++)
#pragma unroll
    for (int j = 0; j < 3; j++) acc[i][j] = (f32x4){0.f, 0.f, 0.f, 0.f};

  const u16* asrc[2];
#pragma unroll
  for (int i = 0; i < 2; i++) {
    int off = (wave + 4 * i) * 1024 + lane * 16;
    int r = off >> 6, pc = (off >> 4) & 3;
    int c = pc ^ ((r >> 2) & 3);
    asrc[i] = x + (size_t)(m0 + r) * D_CH + c * 8;
  }
  const u16* bsrc[2];
  {
    int off = wave * 1024 + lane * 16;
    int r = off / 192, c = (off % 192) >> 4;
    bsrc[0] = w1 + (size_t)r * F_FF + n0 + c * 8;
    int off2 = (4 + (wave & 1)) * 1024 + lane * 16;
    r = off2 / 192; c = (off2 % 192) >> 4;
    bsrc[1] = w1 + (size_t)r * F_FF + n0 + c * 8;
  }
  char* lbase = reinterpret_cast<char*>(&S.st[0][0]);
#define G1_ISSUE(kt_, buf_)                                                                        \
  {                                                                                                \
    int kk = (kt_) < NK ? (kt_) : NK - 1;                                                          \
    char* lb = lbase + (buf_) * G1_STAGE_BYTES;                                                    \
    __builtin_amdgcn_global_load_lds((const unsigned*)(asrc[0] + kk * G1_BK),                      \
                                     (unsigned*)(lb + wave * 1024), 16, 0, 0);                     \
    __builtin_amdgcn_global_load_lds((const unsigned*)(asrc[1] + kk * G1_BK),                      \
                                     (unsigned*)(lb + (wave + 4) * 1024), 16, 0, 0);               \
    __builtin_amdgcn_global_load_lds((const unsigned*)(bsrc[0] + (size_t)kk * G1_BK * F_FF),       \
                                     (unsigned*)(lb + 8192 + wave * 1024), 16, 0, 0);              \
    __builtin_amdgcn_global_load_lds((const unsigned*)(bsrc[1] + (size_t)kk * G1_BK * F_FF),       \
                                     (unsigned*)(wave < 2 ? lb + 8192 + (4 + wave) * 1024          \
                                                          : lbase + 3 * G1_STAGE_BYTES), 16, 0, 0); \
  }
  const int NK = D_CH / G1_BK;
  G1_ISSUE(0, 0);
  G1_ISSUE(1, 1);
  int buf = 0;
  for (int kt = 0; kt < NK; kt++) {
    asm volatile("s_waitcnt vmcnt(4)" ::: "memory");
    asm volatile("s_waitcnt lgkmcnt(0)" ::: "memory");
    __builtin_amdgcn_s_barrier();
    {
      int nb = buf + 2; if (nb >= 3) nb -= 3;
      G1_ISSUE(kt + 2, nb);
    }
    {
      const char* lb = lbase + buf * G1_STAGE_BYTES;
      bf16x8 af[4], bfr[3];
#pragma unroll
      for (int i = 0; i < 4; i++) {
        int row = wm * 64 + i * 16 + (lane & 15);
        int pc = (lane >> 4) ^ ((row >> 2) & 3);
        af[i] = *reinterpret_cast<const bf16x8*>(lb + row * 64 + pc * 16);
      }
#pragma unroll
      for (int j = 0; j < 3; j++) {
        int q = lane >> 4, ii = lane & 15;
        const u16* p0 = reinterpret_cast<const u16*>(lb + 8192) + (8 * q + (ii >> 2)) * 96 + wn * 48 + j * 16 + 4 * (ii & 3);
        s16x4 lo = lds_tr16(p0);
        s16x4 hi = lds_tr16(p0 + 4 * 96);
        bfr[j] = (bf16x8){lo[0], lo[1], lo[2], lo[3], hi[0], hi[1], hi[2], hi[3]};
      }
#pragma unroll
      for (int i = 0; i < 4; i++)
#pragma unroll
        for (int j = 0; j < 3; j++)
          acc[i][j] = __builtin_amdgcn_mfma_f32_16x16x32_bf16(af[i], bfr[j], acc[i][j], 0, 0, 0);
    }
    buf = buf + 1; if (buf >= 3) buf = 0;
  }
  asm volatile("s_waitcnt vmcnt(0)" ::: "memory");
#pragma unroll
  for (int i = 0; i < 4; i++)
#pragma unroll
    for (int j = 0; j < 3; j++) {
      int col = n0 + wn * 48 + j * 16 + (lane & 15);
#pragma unroll
      for (int r = 0; r < 4; r++) {
        int row = m0 + wm * 64 + i * 16 + 4 * (lane >> 4) + r;
        float v = rbf(acc[i][j][r]);
        float s = v / (1.f + __expf(-v));
        h[(size_t)row * F_FF + col] = f2bf(s);
      }
    }
}

__device__ void conv_block(int d, const u16* __restrict__ u, const u16* __restrict__ logAre,
                           const u16* __restrict__ Aim, const u16* __restrict__ Cre,
                           const u16* __restrict__ Cim, const u16* __restrict__ logdt,
                           const u16* __restrict__ Dsk, u16* __restrict__ aout, char* smem) {
  ConvLds& S = *reinterpret_cast<ConvLds*>(smem);
  const int tid = threadIdx.x;
  const int lane = tid & 63;
  const int wave = tid >> 6;

  u16 ureg[8];
#pragma unroll
  for (int i = 0; i < 8; i++) ureg[i] = u[(size_t)(tid + 256 * i) * D_CH + d];
  const float dsk = bf2f(Dsk[d]);
  if (tid < N_ST) {
    int n = tid;
    const float dt = rbf(expf(bf2f(logdt[d])));
    float are = -rbf(expf(bf2f(logAre[d * N_ST + n])));
    float aim = bf2f(Aim[d * N_ST + n]);
    float cre = bf2f(Cre[d * N_ST + n]) * dt;
    float cim = bf2f(Cim[d * N_ST + n]) * dt;
    double al = (double)(are * dt) * 1.4426950408889634;
    double be = (double)(aim * dt) * 0.15915494309189535;
    float beh = (float)be;
    float bel = (float)(be - (double)beh);
    float mag2 = cre * cre + cim * cim;
    float lnc = 0.5f * log2f(mag2) + 1.0f;
    float ph = atan2f(cim, cre) * 0.15915494309189535f;
    S.prm[n][0] = (float)al; S.prm[n][1] = beh; S.prm[n][2] = bel; S.prm[n][3] = lnc; S.prm[n][4] = ph;
  }
  __syncthreads();

  {
    const int r16 = lane & 15, q = lane >> 4;
    const int part = q & 1;
    float pr[3];
    pr[0] = (float)r16;
    pr[1] = (r16 < 8) ? (float)(256 + 2 * r16) : (float)(512 + 4 * (r16 & 3) + 256 * ((r16 - 8) >> 2));
    pr[2] = (r16 < 8) ? (float)(1024 + 8 * (r16 & 1) + 256 * (r16 >> 1)) : 2048.f;
    const float pc = (float)(16 * r16);
    const float qa = 0.25f * (float)part, qb = -0.25f * (float)part;
    f32x4 acc[3];
#pragma unroll
    for (int i = 0; i < 3; i++) acc[i] = (f32x4){0.f, 0.f, 0.f, 0.f};
#pragma unroll 4
    for (int s = 0; s < 16; s++) {
      int n = 32 * wave + 2 * s + (q >> 1);
      f32x4 pv = *reinterpret_cast<const f32x4*>(&S.prm[n][0]);
      float ph = S.prm[n][4];
      float al = pv[0], beh = pv[1], bel = pv[2], lnc = pv[3];
      float bv;
      {
        float t = __builtin_amdgcn_fractf(fmaf(beh, pc, qb));
        t = fmaf(bel, pc, t);
        bv = __builtin_amdgcn_exp2f(al * pc) * __builtin_amdgcn_cosf(t);
      }
#pragma unroll
      for (int i = 0; i < 3; i++) {
        float t = __builtin_amdgcn_fractf(fmaf(beh, pr[i], qa));
        t = fmaf(bel, pr[i], t + ph);
        float av = __builtin_amdgcn_exp2f(fmaf(al, pr[i], lnc)) * __builtin_amdgcn_cosf(t);
        acc[i] = __builtin_amdgcn_mfma_f32_16x16x4f32(av, bv, acc[i], 0, 0, 0);
      }
    }
#pragma unroll
    for (int i = 0; i < 3; i++)
#pragma unroll
      for (int j = 0; j < 4; j++) S.kp[wave][(16 * i + 4 * q + j) * 16 + r16] = acc[i][j];
  }
  S.us[tid] = 0;
#pragma unroll
  for (int i = 0; i < 8; i++) S.us[256 + tid + 256 * i] = ureg[i];
  __syncthreads();
#pragma unroll
  for (int i = 0; i < 3; i++) {
    int e = tid + 256 * i;
    S.kp[0][e] = (S.kp[0][e] + S.kp[1][e]) + (S.kp[2][e] + S.kp[3][e]);
  }
  __syncthreads();
  for (int x = tid; x < CV_R_ELEMS; x += 256) {
    int m = 2047 - x;
    float kv = 0.f;
    if (m >= 0) {
      int p = (int)rbf((float)m);
      int row, col;
      if (p < 256) { row = p & 15; col = p >> 4; }
      else if (p < 512) { row = 16 + ((p & 15) >> 1); col = (p - 256) >> 4; }
      else if (p < 1024) { row = 24 + ((p & 15) >> 2) + 4 * ((p - 512) >> 8); col = ((p - 512) >> 4) & 15; }
      else if (p < 2048) { row = 32 + ((p & 15) >> 3) + 2 * ((p - 1024) >> 8); col = ((p - 1024) >> 4) & 15; }
      else { row = 40; col = 0; }
      kv = S.kp[0][row * 16 + col];
    }
    u16 hi = f2bf(kv);
    u16 lo = f2bf(kv - bf2f(hi));
    S.r[0][x] = hi;
    S.r[2][x] = lo;
    if (x + 1 < CV_R_ELEMS) {
      S.r[1][x + 1] = hi;
      S.r[3][x + 1] = lo;
    }
  }
  __syncthreads();

  const int r16 = lane & 15, q = lane >> 4;
  const int tA = wave, tB = 7 - wave;
  f32x4 accA = {0.f, 0.f, 0.f, 0.f}, accB = {0.f, 0.f, 0.f, 0.f};
  const int kmax = 16 * tB + 15;
  for (int k = 0; k <= kmax; k += 2) {
    int base = 16 * (k + 1) + r16 - 8 * q;
    int xx = 2047 - base;
    const unsigned* rh;
    const unsigned* rl;
    if (xx & 1) {
      rh = reinterpret_cast<const unsigned*>(&S.r[1][xx + 1]);
      rl = reinterpret_cast<const unsigned*>(&S.r[3][xx + 1]);
    } else {
      rh = reinterpret_cast<const unsigned*>(&S.r[0][xx]);
      rl = reinterpret_cast<const unsigned*>(&S.r[2][xx]);
    }
    union { unsigned w[4]; bf16x8 v; } ah, al;
#pragma unroll
    for (int i = 0; i < 4; i++) { ah.w[i] = rh[i]; al.w[i] = rl[i]; }
    {
      int s = 16 * tB - k;
      const u16* bp = &S.us[256 + 16 * (s + r16 - 1) + 8 * q];
      bf16x8 bfr = *reinterpret_cast<const bf16x8*>(bp);
      accB = __builtin_amdgcn_mfma_f32_16x16x32_bf16(ah.v, bfr, accB, 0, 0, 0);
      accB = __builtin_amdgcn_mfma_f32_16x16x32_bf16(al.v, bfr, accB, 0, 0, 0);
    }
    if (k <= 16 * tA + 15) {
      int s = 16 * tA - k;
      const u16* bp = &S.us[256 + 16 * (s + r16 - 1) + 8 * q];
      bf16x8 bfr = *reinterpret_cast<const bf16x8*>(bp);
      accA = __builtin_amdgcn_mfma_f32_16x16x32_bf16(ah.v, bfr, accA, 0, 0, 0);
      accA = __builtin_amdgcn_mfma_f32_16x16x32_bf16(al.v, bfr, accA, 0, 0, 0);
    }
  }
  {
    int l = 16 * (16 * tA + r16) + 4 * q;
    s16x4 o;
#pragma unroll
    for (int r = 0; r < 4; r++) o[r] = (short)f2bf(rbf(accA[r]) + rbf(bf2f(S.us[256 + l + r]) * dsk));
    *reinterpret_cast<s16x4*>(aout + (size_t)d * L_SEQ + l) = o;
    l = 16 * (16 * tB + r16) + 4 * q;
#pragma unroll
    for (int r = 0; r < 4; r++) o[r] = (short)f2bf(rbf(accB[r]) + rbf(bf2f(S.us[256 + l + r]) * dsk));
    *reinterpret_cast<s16x4*>(aout + (size_t)d * L_SEQ + l) = o;
  }
}

__device__ void gate_block(int bid, const u16* __restrict__ x, const u16* __restrict__ gw,
                           float* __restrict__ g) {
  const int lane = threadIdx.x & 63;
  const int wave = threadIdx.x >> 6;
  const unsigned* gw32 = reinterpret_cast<const unsigned*>(gw);
  unsigned gwv[5], xv[4][5];
#pragma unroll
  for (int i = 0; i < 5; i++) gwv[i] = gw32[lane + 64 * i];
#pragma unroll
  for (int rr = 0; rr < 4; rr++) {
    int row = bid * 16 + wave * 4 + rr;
    const unsigned* xr = reinterpret_cast<const unsigned*>(x + (size_t)row * D_CH);
#pragma unroll
    for (int i = 0; i < 5; i++) xv[rr][i] = xr[lane + 64 * i];
  }
#pragma unroll
  for (int rr = 0; rr < 4; rr++) {
    int row = bid * 16 + wave * 4 + rr;
    float s = 0.f;
#pragma unroll
    for (int i = 0; i < 5; i++) {
      s += rbf(bf2f((u16)(xv[rr][i] & 0xffff)) * bf2f((u16)(gwv[i] & 0xffff)));
      s += rbf(bf2f((u16)(xv[rr][i] >> 16)) * bf2f((u16)(gwv[i] >> 16)));
    }
#pragma unroll
    for (int off = 32; off > 0; off >>= 1) s += __shfl_xor(s, off);
    if (lane == 0) {
      float sv = rbf(s);
      g[row] = rbf(1.f / (1.f + __expf(-sv)));
    }
  }
}

__global__ void __launch_bounds__(256) k1(const u16* __restrict__ x, const u16* __restrict__ logAre,
                                          const u16* __restrict__ Aim, const u16* __restrict__ Cre,
                                          const u16* __restrict__ Cim, const u16* __restrict__ logdt,
                                          const u16* __restrict__ Dsk, const u16* __restrict__ w1,
                                          const u16* __restrict__ gw, u16* __restrict__ h,
                                          u16* __restrict__ aout, float* __restrict__ g) {
  __shared__ __align__(16) char smem[sizeof(ConvLds) > sizeof(G1Lds) ? sizeof(ConvLds) : sizeof(G1Lds)];
  int bid = blockIdx.x;
  if (bid < CONV_BLOCKS) {
    conv_block(bid, x, logAre, Aim, Cre, Cim, logdt, Dsk, aout, smem);
  } else if (bid < CONV_BLOCKS + G1_BLOCKS) {
    gemm1_block(bid - CONV_BLOCKS, x, w1, h, smem);
  } else {
    gate_block(bid - CONV_BLOCKS - G1_BLOCKS, x, gw, g);
  }
}

#define G2_BM 64
#define G2_BN 80
#define G2_BK 64
#define G2_LDA 72
#define G2_LDB 88
#define G2_TILES_N (D_CH / G2_BN)
#define G2_TILES_M (L_SEQ / G2_BM)

#define G2_STAGE_BYTES (8192 + 10240)
struct __align__(16) G2Lds {
  union {
    struct {
      u16 st[4][G2_STAGE_BYTES / 2];
      u16 dummy[512];
    } t;
    float red[2][10][64][4];
  } un;
};

__global__ void __launch_bounds__(256) k2(const u16* __restrict__ h, const u16* __restrict__ w2,
                                          const u16* __restrict__ aout, const float* __restrict__ g,
                                          u16* __restrict__ out) {
  __shared__ G2Lds S;
  const int tid = threadIdx.x;
  const int lane = tid & 63;
  const int wave = tid >> 6;
  const int bid = blockIdx.x;
  const int xcd = bid & 7, loc = bid >> 3;
  const int tm = 4 * xcd + (loc >> 3), tn = loc & 7;
  const int m0 = tm * G2_BM, n0 = tn * G2_BN;
  const int wm = wave & 1, wk = wave >> 1;

  f32x4 acc[2][5];
#pragma unroll
  for (int i = 0; i < 2; i++)
#pragma unroll
    for (int j = 0; j < 5; j++) acc[i][j] = (f32x4){0.f, 0.f, 0.f, 0.f};

  const u16* asrc[2];
#pragma unroll
  for (int i = 0; i < 2; i++) {
    int off = (wave + 4 * i) * 1024 + lane * 16;
    int r = off >> 7, pc = (off >> 4) & 7;
    int c = pc ^ (r & 7);
    asrc[i] = h + (size_t)(m0 + r) * F_FF + c * 8;
  }
  const u16* bsrc[3];
#pragma unroll
  for (int i = 0; i < 3; i++) {
    int ins = (i < 2) ? (wave + 4 * i) : (8 + (wave & 1));
    int off = ins * 1024 + lane * 16;
    int r = off / 160, c = (off % 160) >> 4;
    bsrc[i] = w2 + (size_t)r * D_CH + n0 + c * 8;
  }
  char* lbase = reinterpret_cast<char*>(&S.un.t.st[0][0]);
#define G2_ISSUE(kt_, buf_)                                                                        \
  {                                                                                                \
    int kk = (kt_) < NK ? (kt_) : NK - 1;                                                          \
    char* lb = lbase + (buf_) * G2_STAGE_BYTES;                                                    \
    __builtin_amdgcn_global_load_lds((const unsigned*)(asrc[0] + kk * G2_BK),                      \
                                     (unsigned*)(lb + wave * 1024), 16, 0, 0);                     \
    __builtin_amdgcn_global_load_lds((const unsigned*)(asrc[1] + kk * G2_BK),                      \
                                     (unsigned*)(lb + (wave + 4) * 1024), 16, 0, 0);               \
    __builtin_amdgcn_global_load_lds((const unsigned*)(bsrc[0] + (size_t)kk * G2_BK * D_CH),       \
                                     (unsigned*)(lb + 8192 + wave * 1024), 16, 0, 0);              \
    __builtin_amdgcn_global_load_lds((const unsigned*)(bsrc[1] + (size_t)kk * G2_BK * D_CH),       \
                                     (unsigned*)(lb + 8192 + (wave + 4) * 1024), 16, 0, 0);        \
    __builtin_amdgcn_global_load_lds((const unsigned*)(bsrc[2] + (size_t)kk * G2_BK * D_CH),       \
                                     (unsigned*)(wave < 2 ? lb + 8192 + (8 + wave) * 1024          \
                                                          : lbase + 4 * G2_STAGE_BYTES), 16, 0, 0); \
  }
  const int NK = F_FF / G2_BK;
  G2_ISSUE(0, 0);
  G2_ISSUE(1, 1);
  G2_ISSUE(2, 2);
  for (int kt = 0; kt < NK; kt++) {
    asm volatile("s_waitcnt vmcnt(10)" ::: "memory");
    asm volatile("s_waitcnt lgkmcnt(0)" ::: "memory");
    __builtin_amdgcn_s_barrier();
    G2_ISSUE(kt + 3, (kt + 3) & 3);
    {
      const char* lb = lbase + (kt & 3) * G2_STAGE_BYTES;
      const int ks = wk;
      bf16x8 af[2], bfr[5];
#pragma unroll
      for (int i = 0; i < 2; i++) {
        int row = wm * 32 + i * 16 + (lane & 15);
        int pc = (ks * 4 + (lane >> 4)) ^ (row & 7);
        af[i] = *reinterpret_cast<const bf16x8*>(lb + row * 128 + pc * 16);
      }
#pragma unroll
      for (int j = 0; j < 5; j++) {
        int q = lane >> 4, ii = lane & 15;
        const u16* p0 = reinterpret_cast<const u16*>(lb + 8192) + (ks * 32 + 8 * q + (ii >> 2)) * 80 + j * 16 + 4 * (ii & 3);
        s16x4 lo = lds_tr16(p0);
        s16x4 hi = lds_tr16(p0 + 4 * 80);
        bfr[j] = (bf16x8){lo[0], lo[1], lo[2], lo[3], hi[0], hi[1], hi[2], hi[3]};
      }
#pragma unroll
      for (int i = 0; i < 2; i++)
#pragma unroll
        for (int j = 0; j < 5; j++)
          acc[i][j] = __builtin_amdgcn_mfma_f32_16x16x32_bf16(af[i], bfr[j], acc[i][j], 0, 0, 0);
    }
  }
  asm volatile("s_waitcnt vmcnt(0)" ::: "memory");
  __syncthreads();
  if (wk == 1) {
#pragma unroll
    for (int i = 0; i < 2; i++)
#pragma unroll
      for (int j = 0; j < 5; j++)
        *reinterpret_cast<f32x4*>(&S.un.red[wm][i * 5 + j][lane][0]) = acc[i][j];
  }
  __syncthreads();
  if (wk == 0) {
#pragma unroll
    for (int i = 0; i < 2; i++)
#pragma unroll
      for (int j = 0; j < 5; j++) {
        f32x4 o = *reinterpret_cast<const f32x4*>(&S.un.red[wm][i * 5 + j][lane][0]);
        int col = n0 + j * 16 + (lane & 15);
        s16x4 av4 = *reinterpret_cast<const s16x4*>(aout + (size_t)col * L_SEQ + m0 + wm * 32 + i * 16 + 4 * (lane >> 4));
#pragma unroll
        for (int r = 0; r < 4; r++) {
          int row = m0 + wm * 32 + i * 16 + 4 * (lane >> 4) + r;
          float b = rbf(acc[i][j][r] + o[r]);
          float gv = g[row];
          float av = bf2f((u16)av4[r]);
          float t1 = rbf(gv * av);
          float t2 = rbf(rbf(1.f - gv) * b);
          out[(size_t)row * D_CH + col] = f2bf(t1 + t2);
        }
      }
  }
}

extern "C" void kernel_launch(void* const* d_in, const int* in_sizes, int n_in,
                              void* d_out, int out_size, void* d_ws, size_t ws_size,
                              hipStream_t stream) {
  const u16* u = (const u16*)d_in[0];
  const u16* logAre = (const u16*)d_in[1];
  const u16* Aim = (const u16*)d_in[2];
  const u16* Cre = (const u16*)d_in[3];
  const u16* Cim = (const u16*)d_in[4];
  const u16* logdt = (const u16*)d_in[5];
  const u16* Dsk = (const u16*)d_in[6];
  const u16* w1 = (const u16*)d_in[8];
  const u16* w2 = (const u16*)d_in[9];
  const u16* gw = (const u16*)d_in[10];
  u16* out = (u16*)d_out;
  char* ws = (char*)d_ws;
  u16* h = (u16*)ws;
  u16* aout = (u16*)(ws + (8u << 20));
  float* g = (float*)(ws + (12u << 20));

  k1<<<CONV_BLOCKS + G1_BLOCKS + GATE_BLOCKS, 256, 0, stream>>>(u, logAre, Aim, Cre, Cim, logdt, Dsk,
                                                                w1, gw, h, aout, g);
  k2<<<G2_TILES_M * G2_TILES_N, 256, 0, stream>>>(h, w2, aout, g, out);
}
```

```cpp
#include <hip/hip_runtime.h>
#include <hip/hip_bf16.h>
#include <stdint.h>

#define L_SEQ 2048
#define D_CH 640
#define N_ST 128
#define F_FF 1728

typedef unsigned short u16;
typedef __attribute__((ext_vector_type(8))) short bf16x8;
typedef __attribute__((ext_vector_type(4))) short s16x4;
typedef __attribute__((ext_vector_type(4))) float f32x4;
typedef __attribute__((ext_vector_type(4))) unsigned u32x4;

__device__ __forceinline__ float bf2f(u16 v) { return __uint_as_float(((unsigned)v) << 16); }
__device__ __forceinline__ u16 f2bf(float f) {
  unsigned u = __float_as_uint(f);
  u += 0x7FFFu + ((u >> 16) & 1u);
  return (u16)(u >> 16);
}
__device__ __forceinline__ float rbf(float f) { return bf2f(f2bf(f)); }

__device__ __forceinline__ s16x4 lds_tr16(const u16* p) {
  return __builtin_amdgcn_ds_read_tr16_b64_v4i16((__attribute__((address_space(3))) s16x4*)(p));
}

#define K1_THREADS 512
#define G1_BM 128
#define G1_BN 96
#define G1_BK 32
#define G1_TILES_N (F_FF / G1_BN)
#define G1_TILES_M (L_SEQ / G1_BM)
#define G1_BLOCKS (G1_TILES_M * G1_TILES_N)
#define G1_NST 5
#define G1_NK (D_CH / G1_BK)
#define G1_OLD 104
#define CONV_BLOCKS (D_CH / 4)
#define GATE_BLOCKS 64

#define CV_US_ELEMS (256 + L_SEQ)
#define CV_R_ELEMS 2080

struct __align__(16) ConvLds {
  u16 us[4][CV_US_ELEMS];
  f32x4 prmA[4][N_ST];
  float prmB[4][N_ST];
  float ksum[2][768];
  union {
    float kp[2][4][768];
    u16 r[2][4][CV_R_ELEMS];
  } un;
};

#define G1_STAGE_BYTES (8192 + 6144)
struct __align__(16) G1Lds {
  union {
    u16 st[G1_NST][G1_STAGE_BYTES / 2];
    u16 ot[G1_BM][G1_OLD];
  } un;
};

__device__ void gemm1_block(int bid, const u16* __restrict__ x, const u16* __restrict__ w1,
                            u16* __restrict__ h, char* smem) {
  G1Lds& S = *reinterpret_cast<G1Lds*>(smem);
  const int tid = threadIdx.x;
  const int lane = tid & 63;
  const int wave = tid >> 6;
  const int xcd = bid & 7, loc = bid >> 3;
  const int tm = 2 * xcd + loc / G1_TILES_N, tn = loc % G1_TILES_N;
  const int m0 = tm * G1_BM, n0 = tn * G1_BN;
  const int rot = (tn + 10 * (tm & 1)) % G1_NK;
  char* lbase = reinterpret_cast<char*>(&S.un.st[0][0]);

  if (wave >= 4) {
    const int ld = wave - 4;
    const u16* asrc;
    {
      int off = ld * 1024 + lane * 16;
      int r = off >> 6, pc = (off >> 4) & 3;
      int c = pc ^ ((0x78 >> (2 * ((r >> 2) & 3))) & 3);
      asrc = x + (size_t)(m0 + r) * D_CH + c * 8;
    }
    const u16* bsrc[2];
#pragma unroll
    for (int i = 0; i < 2; i++) {
      int off = (ld + 4 * i) * 1024 + lane * 16;
      if (off >= 6144) off = lane * 16;
      int r = off / 192, cp = (off % 192) >> 4;
      int c = cp ^ (2 * ((r >> 3) & 1));
      bsrc[i] = w1 + (size_t)r * F_FF + n0 + c * 8;
    }
#define G1_ISSUE(it_, buf_)                                                                        \
  {                                                                                                \
    int kk = (it_) + rot; if (kk >= G1_NK) kk -= G1_NK;                                            \
    char* lb = lbase + (buf_) * G1_STAGE_BYTES;                                                    \
    __builtin_amdgcn_global_load_lds((const unsigned*)(asrc + kk * G1_BK),                         \
                                     (unsigned*)(lb + ld * 1024), 16, 0, 0);                       \
    __builtin_amdgcn_global_load_lds((const unsigned*)(asrc + (size_t)64 * D_CH + kk * G1_BK),     \
                                     (unsigned*)(lb + (ld + 4) * 1024), 16, 0, 0);                 \
    __builtin_amdgcn_global_load_lds((const unsigned*)(bsrc[0] + (size_t)kk * G1_BK * F_FF),       \
                                     (unsigned*)(lb + 8192 + ld * 1024), 16, 0, 0);                \
    if (ld < 2)                                                                                    \
      __builtin_amdgcn_global_load_lds((const unsigned*)(bsrc[1] + (size_t)kk * G1_BK * F_FF),     \
                                       (unsigned*)(lb + 8192 + (ld + 4) * 1024), 16, 0, 0);        \
  }
#pragma unroll
    for (int s = 0; s < G1_NST - 1; s++) G1_ISSUE(s, s);
    if (ld < 2) asm volatile("s_waitcnt vmcnt(12)" ::: "memory");
    else asm volatile("s_waitcnt vmcnt(9)" ::: "memory");
    __builtin_amdgcn_s_barrier();
    int bp = G1_NST - 1;
    for (int it = 0; it < G1_NK; it++) {
      if (ld < 2) asm volatile("s_waitcnt vmcnt(8)" ::: "memory");
      else asm volatile("s_waitcnt vmcnt(6)" ::: "memory");
      __builtin_amdgcn_s_barrier();
      int nx = it + G1_NST - 1;
      if (nx < G1_NK) { G1_ISSUE(nx, bp); }
      else asm volatile("s_waitcnt vmcnt(0)" ::: "memory");
      bp = bp + 1; if (bp >= G1_NST) bp = 0;
    }
    asm volatile("s_waitcnt vmcnt(0)" ::: "memory");
    __syncthreads();
  } else {
    const int wm = wave & 1, wn = wave >> 1;
    f32x4 acc[4][3];
#pragma unroll
    for (int i = 0; i < 4; i++)
#pragma unroll
      for (int j = 0; j < 3; j++) acc[i][j] = (f32x4){0.f, 0.f, 0.f, 0.f};
    const int q = lane >> 4, ii = lane & 15;
    const int aoff = (wm * 64 + ii) * 64 + ((q ^ ((0x78 >> (2 * ((ii >> 2) & 3))) & 3)) * 16);
    const int brow = (8 * q + (ii >> 2)) * 192 + 8 * (ii & 3);
#define G1_FRAGS(AF, BF, buf_)                                                                     \
  {                                                                                                \
    const char* lb = lbase + (buf_) * G1_STAGE_BYTES;                                              \
    _Pragma("unroll") for (int i = 0; i < 4; i++)                                                  \
      AF[i] = *reinterpret_cast<const bf16x8*>(lb + aoff + i * 1024);                              \
    _Pragma("unroll") for (int j = 0; j < 3; j++) {                                                \
      const char* p0 = lb + 8192 + brow + (((wn * 3 + j) ^ (q & 1)) * 32);                         \
      s16x4 lo = lds_tr16(reinterpret_cast<const u16*>(p0));                                       \
      s16x4 hi = lds_tr16(reinterpret_cast<const u16*>(p0 + 4 * 192));                             \
      BF[j] = (bf16x8){lo[0], lo[1], lo[2], lo[3], hi[0], hi[1], hi[2], hi[3]};                    \
    }                                                                                              \
  }
    bf16x8 af[4], bfr[3], an[4], bn[3];
    __builtin_amdgcn_s_barrier();
    G1_FRAGS(af, bfr, 0);
    int b1 = 1;
    for (int it = 0; it < G1_NK; it++) {
      asm volatile("s_waitcnt lgkmcnt(0)" ::: "memory");
      __builtin_amdgcn_s_barrier();
      G1_FRAGS(an, bn, b1);
#pragma unroll
      for (int i = 0; i < 4; i++)
#pragma unroll
        for (int j = 0; j < 3; j++)
          acc[i][j] = __builtin_amdgcn_mfma_f32_16x16x32_bf16(af[i], bfr[j], acc[i][j], 0, 0, 0);
#pragma unroll
      for (int i = 0; i < 4; i++) af[i] = an[i];
#pragma unroll
      for (int j = 0; j < 3; j++) bfr[j] = bn[j];
      b1 = b1 + 1; if (b1 >= G1_NST) b1 = 0;
    }
    __syncthreads();
#pragma unroll
    for (int i = 0; i < 4; i++)
#pragma unroll
      for (int j = 0; j < 3; j++)
#pragma unroll
        for (int r = 0; r < 4; r++) {
          float v = rbf(acc[i][j][r]);
          float s = v / (1.f + __expf(-v));
          S.un.ot[wm * 64 + i * 16 + 4 * q + r][wn * 48 + j * 16 + ii] = f2bf(s);
        }
  }
  __syncthreads();
#pragma unroll
  for (int i = 0; i < 3; i++) {
    int c = tid + K1_THREADS * i;
    int r = c / 12, cc = c - r * 12;
    u32x4 v = *reinterpret_cast<const u32x4*>(&S.un.ot[r][cc * 8]);
    *reinterpret_cast<u32x4*>(h + (size_t)(m0 + r) * F_FF + n0 + cc * 8) = v;
  }
}

__device__ void conv_block(int blk, const u16* __restrict__ u, const u16* __restrict__ logAre,
                           const u16* __restrict__ Aim, const u16* __restrict__ Cre,
                           const u16* __restrict__ Cim, const u16* __restrict__ logdt,
                           const u16* __restrict__ Dsk, u16* __restrict__ aout, char* smem) {
  ConvLds& S = *reinterpret_cast<ConvLds*>(smem);
  const int tid = threadIdx.x;
  const int lane = tid & 63;
  const int wave = tid >> 6;
  const int cg = wave >> 2, w4 = wave & 3, gt = tid & 255;
  const int d0 = 4 * blk;

  typedef __attribute__((ext_vector_type(2))) unsigned u32x2;
  u32x2 ureg[4];
#pragma unroll
  for (int i = 0; i < 4; i++)
    ureg[i] = *reinterpret_cast<const u32x2*>(u + (size_t)(tid + K1_THREADS * i) * D_CH + d0);
  {
    const int ch = tid >> 7, n = tid & 127, d = d0 + ch;
    const float dt = rbf(expf(bf2f(logdt[d])));
    float are = -rbf(expf(bf2f(logAre[d * N_ST + n])));
    float aim = bf2f(Aim[d * N_ST + n]);
    float cre = bf2f(Cre[d * N_ST + n]) * dt;
    float cim = bf2f(Cim[d * N_ST + n]) * dt;
    double al = (double)(are * dt) * 1.4426950408889634;
    double be = (double)(aim * dt) * 0.15915494309189535;
    float beh = (float)be;
    float bel = (float)(be - (double)beh);
    float mag2 = cre * cre + cim * cim;
    float lnc = 0.5f * log2f(mag2) + 1.0f;
    float ph = atan2f(cim, cre) * 0.15915494309189535f;
    S.prmA[ch][n] = (f32x4){(float)al, beh, bel, lnc};
    S.prmB[ch][n] = ph;
  }
  __syncthreads();

  const int r16 = lane & 15, q = lane >> 4;
  for (int pass = 0; pass < 2; pass++) {
    const int ch = 2 * pass + cg;
    {
      const int part = q & 1;
      float pr[3];
      pr[0] = (float)r16;
      pr[1] = (r16 < 8) ? (float)(256 + 2 * r16) : (float)(512 + 4 * (r16 & 3) + 256 * ((r16 - 8) >> 2));
      pr[2] = (r16 < 8) ? (float)(1024 + 8 * (r16 & 1) + 256 * (r16 >> 1)) : 2048.f;
      const float pc = (float)(16 * r16);
      const float qa = 0.25f * (float)part, qb = -0.25f * (float)part;
      f32x4 acc[3];
#pragma unroll
      for (int i = 0; i < 3; i++) acc[i] = (f32x4){0.f, 0.f, 0.f, 0.f};
#pragma unroll 4
      for (int s = 0; s < 16; s++) {
        int n = 32 * w4 + 2 * s + (q >> 1);
        f32x4 pv = S.prmA[ch][n];
        float ph = S.prmB[ch][n];
        float al = pv[0], beh = pv[1], bel = pv[2], lnc = pv[3];
        float bv;
        {
          float t = __builtin_amdgcn_fractf(fmaf(beh, pc, qb));
          t = fmaf(bel, pc, t);
          bv = __builtin_amdgcn_exp2f(al * pc) * __builtin_amdgcn_cosf(t);
        }
#pragma unroll
        for (int i = 0; i < 3; i++) {
          float t = __builtin_amdgcn_fractf(fmaf(beh, pr[i], qa));
          t = fmaf(bel, pr[i], t + ph);
          float av = __builtin_amdgcn_exp2f(fmaf(al, pr[i], lnc)) * __builtin_amdgcn_cosf(t);
          acc[i] = __builtin_amdgcn_mfma_f32_16x16x4f32(av, bv, acc[i], 0, 0, 0);
        }
      }
#pragma unroll
      for (int i = 0; i < 3; i++)
#pragma unroll
        for (int j = 0; j < 4; j++) S.un.kp[cg][w4][(16 * i + 4 * q + j) * 16 + r16] = acc[i][j];
    }
    if (pass == 0) {
      if (tid < 256) {
#pragma unroll
        for (int c = 0; c < 4; c++) S.us[c][tid] = 0;
      }
#pragma unroll
      for (int i = 0; i < 4; i++) {
        int l = 256 + tid + K1_THREADS * i;
        S.us[0][l] = (u16)(ureg[i][0] & 0xffff);
        S.us[1][l] = (u16)(ureg[i][0] >> 16);
        S.us[2][l] = (u16)(ureg[i][1] & 0xffff);
        S.us[3][l] = (u16)(ureg[i][1] >> 16);
      }
    }
    __syncthreads();
#pragma unroll
    for (int i = 0; i < 3; i++) {
      int e = gt + 256 * i;
      S.ksum[cg][e] = (S.un.kp[cg][0][e] + S.un.kp[cg][1][e]) + (S.un.kp[cg][2][e] + S.un.kp[cg][3][e]);
    }
    __syncthreads();
    for (int x = gt; x < CV_R_ELEMS; x += 256) {
      int m = 2047 - x;
      float kv = 0.f;
      if (m >= 0) {
        int p = (int)rbf((float)m);
        int row, col;
        if (p < 256) { row = p & 15; col = p >> 4; }
        else if (p < 512) { row = 16 + ((p & 15) >> 1); col = (p - 256) >> 4; }
        else if (p < 1024) { row = 24 + ((p & 15) >> 2) + 4 * ((p - 512) >> 8); col = ((p - 512) >> 4) & 15; }
        else if (p < 2048) { row = 32 + ((p & 15) >> 3) + 2 * ((p - 1024) >> 8); col = ((p - 1024) >> 4) & 15; }
        else { row = 40; col = 0; }
        kv = S.ksum[cg][row * 16 + col];
      }
      u16 hi = f2bf(kv);
      u16 lo = f2bf(kv - bf2f(hi));
      S.un.r[cg][0][x] = hi;
      S.un.r[cg][2][x] = lo;
      if (x + 1 < CV_R_ELEMS) {
        S.un.r[cg][1][x + 1] = hi;
        S.un.r[cg][3][x + 1] = lo;
      }
    }
    __syncthreads();

    const u16* us = S.us[ch];
    const int tA = w4, tB = 7 - w4;
    f32x4 accA = {0.f, 0.f, 0.f, 0.f}, accB = {0.f, 0.f, 0.f, 0.f};
    const int kmax = 16 * tB + 15;
    for (int k = 0; k <= kmax; k += 2) {
      int base = 16 * (k + 1) + r16 - 8 * q;
      int xx = 2047 - base;
      const unsigned* rh;
      const unsigned* rl;
      if (xx & 1) {
        rh = reinterpret_cast<const unsigned*>(&S.un.r[cg][1][xx + 1]);
        rl = reinterpret_cast<const unsigned*>(&S.un.r[cg][3][xx + 1]);
      } else {
        rh = reinterpret_cast<const unsigned*>(&S.un.r[cg][0][xx]);
        rl = reinterpret_cast<const unsigned*>(&S.un.r[cg][2][xx]);
      }
      union { unsigned w[4]; bf16x8 v; } ah, al;
#pragma unroll
      for (int i = 0; i < 4; i++) { ah.w[i] = rh[i]; al.w[i] = rl[i]; }
      {
        int s = 16 * tB - k;
        const u16* bp = &us[256 + 16 * (s + r16 - 1) + 8 * q];
        bf16x8 bfr = *reinterpret_cast<const bf16x8*>(bp);
        accB = __builtin_amdgcn_mfma_f32_16x16x32_bf16(ah.v, bfr, accB, 0, 0, 0);
        accB = __builtin_amdgcn_mfma_f32_16x16x32_bf16(al.v, bfr, accB, 0, 0, 0);
      }
      if (k <= 16 * tA + 15) {
        int s = 16 * tA - k;
        const u16* bp = &us[256 + 16 * (s + r16 - 1) + 8 * q];
        bf16x8 bfr = *reinterpret_cast<const bf16x8*>(bp);
        accA = __builtin_amdgcn_mfma_f32_16x16x32_bf16(ah.v, bfr, accA, 0, 0, 0);
        accA = __builtin_amdgcn_mfma_f32_16x16x32_bf16(al.v, bfr, accA, 0, 0, 0);
      }
    }
    {
      const float dsk = bf2f(Dsk[d0 + ch]);
      int l = 16 * (16 * tA + r16) + 4 * q;
      s16x4 o;
#pragma unroll
      for (int r = 0; r < 4; r++) o[r] = (short)f2bf(rbf(accA[r]) + rbf(bf2f(us[256 + l + r]) * dsk));
      *reinterpret_cast<s16x4*>(aout + (size_t)(d0 + ch) * L_SEQ + l) = o;
      l = 16 * (16 * tB + r16) + 4 * q;
#pragma unroll
      for (int r = 0; r < 4; r++) o[r] = (short)f2bf(rbf(accB[r]) + rbf(bf2f(us[256 + l + r]) * dsk));
      *reinterpret_cast<s16x4*>(aout + (size_t)(d0 + ch) * L_SEQ + l) = o;
    }
    __syncthreads();
  }
}

__device__ void gate_block(int bid, const u16* __restrict__ x, const u16* __restrict__ gw,
                           float* __restrict__ g) {
  const int lane = threadIdx.x & 63;
  const int wave = threadIdx.x >> 6;
  const unsigned* gw32 = reinterpret_cast<const unsigned*>(gw);
  unsigned gwv[5], xv[4][5];
#pragma unroll
  for (int i = 0; i < 5; i++) gwv[i] = gw32[lane + 64 * i];
#pragma unroll
  for (int rr = 0; rr < 4; rr++) {
    int row = bid * 32 + wave * 4 + rr;
    const unsigned* xr = reinterpret_cast<const unsigned*>(x + (size_t)row * D_CH);
#pragma unroll
    for (int i = 0; i < 5; i++) xv[rr][i] = xr[lane + 64 * i];
  }
#pragma unroll
  for (int rr = 0; rr < 4; rr++) {
    int row = bid * 32 + wave * 4 + rr;
    float s = 0.f;
#pragma unroll
    for (int i = 0; i < 5; i++) {
      s += rbf(bf2f((u16)(xv[rr][i] & 0xffff)) * bf2f((u16)(gwv[i] & 0xffff)));
      s += rbf(bf2f((u16)(xv[rr][i] >> 16)) * bf2f((u16)(gwv[i] >> 16)));
    }
#pragma unroll
    for (int off = 32; off > 0; off >>= 1) s += __shfl_xor(s, off);
    if (lane == 0) {
      float sv = rbf(s);
      g[row] = rbf(1.f / (1.f + __expf(-sv)));
    }
  }
}

__global__ void __launch_bounds__(K1_THREADS) k1(const u16* __restrict__ x, const u16* __restrict__ logAre,
                                          const u16* __restrict__ Aim, const u16* __restrict__ Cre,
                                          const u16* __restrict__ Cim, const u16* __restrict__ logdt,
                                          const u16* __restrict__ Dsk, const u16* __restrict__ w1,
                                          const u16* __restrict__ gw, u16* __restrict__ h,
                                          u16* __restrict__ aout, float* __restrict__ g) {
  __shared__ __align__(16) char smem[sizeof(ConvLds) > sizeof(G1Lds) ? sizeof(ConvLds) : sizeof(G1Lds)];
  int bid = blockIdx.x;
  if (bid < CONV_BLOCKS) {
    conv_block(bid, x, logAre, Aim, Cre, Cim, logdt, Dsk, aout, smem);
  } else if (bid < CONV_BLOCKS + G1_BLOCKS) {
    gemm1_block(bid - CONV_BLOCKS, x, w1, h, smem);
  } else {
    gate_block(bid - CONV_BLOCKS - G1_BLOCKS, x, gw, g);
  }
}

#define G2_BM 64
#define G2_BN 80
#define G2_BK 64
#define G2_TILES_N (D_CH / G2_BN)
#define G2_TILES_M (L_SEQ / G2_BM)
#define G2_NST 7
#define G2_NK (F_FF / G2_BK)
#define G2_OLD 88

#define G2_STAGE_BYTES (8192 + 10240)
struct __align__(16) G2Lds {
  union {
    u16 st[G2_NST][G2_STAGE_BYTES / 2];
    struct {
      float red[2][2][5][64][4];
      u16 ot[G2_BM][G2_OLD];
    } e;
  } un;
};

__global__ void __launch_bounds__(384) k2(const u16* __restrict__ h, const u16* __restrict__ w2,
                                          const u16* __restrict__ aout, const float* __restrict__ g,
                                          u16* __restrict__ out) {
  __shared__ G2Lds S;
  const int tid = threadIdx.x;
  const int lane = tid & 63;
  const int wave = tid >> 6;
  const int bid = blockIdx.x;
  const int xcd = bid & 7, loc = bid >> 3;
  const int tm = 4 * xcd + (loc >> 3), tn = loc & 7;
  const int m0 = tm * G2_BM, n0 = tn * G2_BN;
  const int rot = (3 * tn + 7 * (tm & 3)) % G2_NK;
  char* lbase = reinterpret_cast<char*>(&S.un.st[0][0]);
#define STAMP(i)

  if (wave >= 4) {
    const int ld = wave - 4;
    const u16* asrc;
    {
      int off = ld * 4096 + lane * 16;
      int r = off >> 7, pc = (off >> 4) & 7;
      int c = pc ^ (r & 7);
      asrc = h + (size_t)(m0 + r) * F_FF + c * 8;
    }
    const u16* bsrc[5];
#pragma unroll
    for (int i = 0; i < 5; i++) {
      int off = (5 * ld + i) * 1024 + lane * 16;
      int r = off / 160, c = (off % 160) >> 4;
      bsrc[i] = w2 + (size_t)r * D_CH + n0 + c * 8;
    }
#define G2_ISSUE(it_, buf_)                                                                        \
  {                                                                                                \
    int kk = (it_) + rot; if (kk >= G2_NK) kk -= G2_NK;                                            \
    char* lb = lbase + (buf_) * G2_STAGE_BYTES;                                                    \
    _Pragma("unroll") for (int i = 0; i < 4; i++)                                                  \
      __builtin_amdgcn_global_load_lds((const unsigned*)(asrc + (size_t)i * 8 * F_FF + kk * G2_BK), \
                                       (unsigned*)(lb + (4 * ld + i) * 1024), 16, 0, 0);           \
    _Pragma("unroll") for (int i = 0; i < 5; i++)                                                  \
      __builtin_amdgcn_global_load_lds((const unsigned*)(bsrc[i] + (size_t)kk * G2_BK * D_CH),     \
                                       (unsigned*)(lb + 8192 + (5 * ld + i) * 1024), 16, 0, 0);    \
  }
#pragma unroll
    for (int s = 0; s < G2_NST - 1; s++) G2_ISSUE(s, s);
    asm volatile("s_waitcnt vmcnt(45)" ::: "memory");
    __builtin_amdgcn_s_barrier();
    int bp = G2_NST - 1;
    for (int it = 0; it < G2_NK; it++) {
      asm volatile("s_waitcnt vmcnt(36)" ::: "memory");
      __builtin_amdgcn_s_barrier();
      int nx = it + G2_NST - 1;
      if (nx < G2_NK) { G2_ISSUE(nx, bp); }
      else asm volatile("s_waitcnt vmcnt(0)" ::: "memory");
      bp = bp + 1; if (bp >= G2_NST) bp = 0;
    }
    asm volatile("s_waitcnt vmcnt(0)" ::: "memory");
    __syncthreads();
    __syncthreads();
  } else {
    const int wm = wave & 1, wk = wave >> 1;
    f32x4 acc[2][5];
#pragma unroll
    for (int i = 0; i < 2; i++)
#pragma unroll
      for (int j = 0; j < 5; j++) acc[i][j] = (f32x4){0.f, 0.f, 0.f, 0.f};
    const int q = lane >> 4, ii = lane & 15;
    const int boff0 = 8192 + ((wk * 32 + 8 * q + (ii >> 2) + 4 * (q & 1)) * 80 + 4 * (ii & 3)) * 2;
    const int boff1 = 8192 + ((wk * 32 + 8 * q + (ii >> 2) + 4 * ((q & 1) ^ 1)) * 80 + 4 * (ii & 3)) * 2;
#define G2_FRAGS(AF, BF, buf_)                                                                     \
  {                                                                                                \
    const char* lb = lbase + (buf_) * G2_STAGE_BYTES;                                              \
    _Pragma("unroll") for (int i = 0; i < 2; i++) {                                                \
      int row = wm * 32 + i * 16 + (lane & 15);                                                    \
      int pc = (wk * 4 + (lane >> 4)) ^ (row & 7);                                                 \
      AF[i] = *reinterpret_cast<const bf16x8*>(lb + row * 128 + pc * 16);                          \
    }                                                                                              \
    _Pragma("unroll") for (int j = 0; j < 5; j++) {                                                \
      s16x4 x0 = lds_tr16(reinterpret_cast<const u16*>(lb + boff0 + j * 32));                      \
      s16x4 x1 = lds_tr16(reinterpret_cast<const u16*>(lb + boff1 + j * 32));                      \
      s16x4 lo = (q & 1) ? x1 : x0, hi = (q & 1) ? x0 : x1;                                        \
      BF[j] = (bf16x8){lo[0], lo[1], lo[2], lo[3], hi[0], hi[1], hi[2], hi[3]};                    \
    }                                                                                              \
  }
    s16x4 av4[5];
    f32x4 g4;
    const int row0 = m0 + wm * 32 + wk * 16 + 4 * q;
#pragma unroll
    for (int j = 0; j < 5; j++)
      av4[j] = *reinterpret_cast<const s16x4*>(aout + (size_t)(n0 + j * 16 + ii) * L_SEQ + row0);
    g4 = *reinterpret_cast<const f32x4*>(g + row0);

    bf16x8 af[2], bfr[5], an[2], bn[5];
    __builtin_amdgcn_s_barrier();
    STAMP(1);
    G2_FRAGS(af, bfr, 0);
    int b1 = 1;
    for (int it = 0; it < G2_NK; it++) {
      asm volatile("s_waitcnt lgkmcnt(0)" ::: "memory");
      __builtin_amdgcn_s_barrier();
      G2_FRAGS(an, bn, b1);
#pragma unroll
      for (int i = 0; i < 2; i++)
#pragma unroll
        for (int j = 0; j < 5; j++)
          acc[i][j] = __builtin_amdgcn_mfma_f32_16x16x32_bf16(af[i], bfr[j], acc[i][j], 0, 0, 0);
#pragma unroll
      for (int i = 0; i < 2; i++) af[i] = an[i];
#pragma unroll
      for (int j = 0; j < 5; j++) bfr[j] = bn[j];
      b1 = b1 + 1; if (b1 >= G2_NST) b1 = 0;
    }
    STAMP(2);
    __syncthreads();
#pragma unroll
    for (int j = 0; j < 5; j++)
      *reinterpret_cast<f32x4*>(&S.un.e.red[wm][wk][j][lane][0]) = wk ? acc[0][j] : acc[1][j];
    __syncthreads();
#pragma unroll
    for (int j = 0; j < 5; j++) {
      f32x4 o = *reinterpret_cast<const f32x4*>(&S.un.e.red[wm][wk ^ 1][j][lane][0]);
      f32x4 mine = wk ? acc[1][j] : acc[0][j];
#pragma unroll
      for (int r = 0; r < 4; r++) {
        float b = rbf(mine[r] + o[r]);
        float av = bf2f((u16)av4[j][r]);
        float t1 = rbf(g4[r] * av);
        float t2 = rbf(rbf(1.f - g4[r]) * b);
        S.un.e.ot[wm * 32 + wk * 16 + 4 * q + r][j * 16 + ii] = f2bf(t1 + t2);
      }
    }
  }
  __syncthreads();
  for (int c = tid; c < 640; c += 384) {
    int r = c / 10, cc = c - r * 10;
    u32x4 v = *reinterpret_cast<const u32x4*>(&S.un.e.ot[r][cc * 8]);
    *reinterpret_cast<u32x4*>(out + (size_t)(m0 + r) * D_CH + n0 + cc * 8) = v;
  }
  STAMP(3);
}

extern "C" void kernel_launch(void* const* d_in, const int* in_sizes, int n_in,
                              void* d_out, int out_size, void* d_ws, size_t ws_size,
                              hipStream_t stream) {
  const u16* u = (const u16*)d_in[0];
  const u16* logAre = (const u16*)d_in[1];
  const u16* Aim = (const u16*)d_in[2];
  const u16* Cre = (const u16*)d_in[3];
  const u16* Cim = (const u16*)d_in[4];
  const u16* logdt = (const u16*)d_in[5];
  const u16* Dsk = (const u16*)d_in[6];
  const u16* w1 = (const u16*)d_in[8];
  const u16* w2 = (const u16*)d_in[9];
  const u16* gw = (const u16*)d_in[10];
  u16* out = (u16*)d_out;
  char* ws = (char*)d_ws;
  u16* h = (u16*)ws;
  u16* aout = (u16*)(ws + (8u << 20));
  float* g = (float*)(ws + (12u << 20));

  k1<<<CONV_BLOCKS + G1_BLOCKS + GATE_BLOCKS, K1_THREADS, 0, stream>>>(u, logAre, Aim, Cre, Cim, logdt, Dsk,
                                                                w1, gw, h, aout, g);
  k2<<<G2_TILES_M * G2_TILES_N, 384, 0, stream>>>(h, w2, aout, g, out);
}
```

```cpp
#include <hip/hip_runtime.h>
#include <hip/hip_bf16.h>
#include <stdint.h>

#define L_SEQ 2048
#define D_CH 640
#define N_ST 128
#define F_FF 1728

typedef unsigned short u16;
#define STAMP1(base, i)
typedef __attribute__((ext_vector_type(8))) short bf16x8;
typedef __attribute__((ext_vector_type(4))) short s16x4;
typedef __attribute__((ext_vector_type(4))) float f32x4;
typedef __attribute__((ext_vector_type(4))) unsigned u32x4;

__device__ __forceinline__ float bf2f(u16 v) { return __uint_as_float(((unsigned)v) << 16); }
__device__ __forceinline__ u16 f2bf(float f) {
  unsigned u = __float_as_uint(f);
  u += 0x7FFFu + ((u >> 16) & 1u);
  return (u16)(u >> 16);
}
__device__ __forceinline__ float rbf(float f) { return bf2f(f2bf(f)); }

__device__ __forceinline__ s16x4 lds_tr16(const u16* p) {
  return __builtin_amdgcn_ds_read_tr16_b64_v4i16((__attribute__((address_space(3))) s16x4*)(p));
}

#define K1_THREADS 512
#define G1_BM 128
#define G1_BN 96
#define G1_BK 32
#define G1_TILES_N (F_FF / G1_BN)
#define G1_TILES_M (L_SEQ / G1_BM)
#define G1_BLOCKS (G1_TILES_M * G1_TILES_N)
#define G1_NST 5
#define G1_NK (D_CH / G1_BK)
#define G1_OLD 104
#define CONV_BLOCKS (D_CH / 2)
#define GATE_BLOCKS 64

#define CV_US_ELEMS (256 + L_SEQ)
#define CV_R_ELEMS 2080

struct __align__(16) ConvLds {
  u16 us[2][CV_US_ELEMS];
  f32x4 prmA[2][N_ST];
  float prmB[2][N_ST];
  float ksum[2][768];
  union {
    float kp[2][4][768];
    u16 r[2][4][CV_R_ELEMS];
  } un;
};

#define G1_STAGE_BYTES (8192 + 6144)
struct __align__(16) G1Lds {
  union {
    u16 st[G1_NST][G1_STAGE_BYTES / 2];
    u16 ot[G1_BM][G1_OLD];
  } un;
};

__device__ void gemm1_block(int bid, const u16* __restrict__ x, const u16* __restrict__ w1,
                            u16* __restrict__ h, char* smem) {
  G1Lds& S = *reinterpret_cast<G1Lds*>(smem);
  const int tid = threadIdx.x;
  const int lane = tid & 63;
  const int wave = tid >> 6;
  const int xcd = bid & 7, loc = bid >> 3;
  const int tm = 2 * xcd + loc / G1_TILES_N, tn = loc % G1_TILES_N;
  const int m0 = tm * G1_BM, n0 = tn * G1_BN;
  const int rot = (tn + 10 * (tm & 1)) % G1_NK;
  char* lbase = reinterpret_cast<char*>(&S.un.st[0][0]);
  STAMP1(h, 0);

  if (wave >= 4) {
    const int ld = wave - 4;
    const u16* asrc;
    {
      int off = ld * 1024 + lane * 16;
      int r = off >> 6, pc = (off >> 4) & 3;
      int c = pc ^ ((0x78 >> (2 * ((r >> 2) & 3))) & 3);
      asrc = x + (size_t)(m0 + r) * D_CH + c * 8;
    }
    const u16* bsrc[2];
#pragma unroll
    for (int i = 0; i < 2; i++) {
      int off = (ld + 4 * i) * 1024 + lane * 16;
      if (off >= 6144) off = lane * 16;
      int r = off / 192, cp = (off % 192) >> 4;
      int c = cp ^ (2 * ((r >> 3) & 1));
      bsrc[i] = w1 + (size_t)r * F_FF + n0 + c * 8;
    }
#define G1_ISSUE(it_, buf_)                                                                        \
  {                                                                                                \
    int kk = (it_) + rot; if (kk >= G1_NK) kk -= G1_NK;                                            \
    char* lb = lbase + (buf_) * G1_STAGE_BYTES;                                                    \
    __builtin_amdgcn_global_load_lds((const unsigned*)(asrc + kk * G1_BK),                         \
                                     (unsigned*)(lb + ld * 1024), 16, 0, 0);                       \
    __builtin_amdgcn_global_load_lds((const unsigned*)(asrc + (size_t)64 * D_CH + kk * G1_BK),     \
                                     (unsigned*)(lb + (ld + 4) * 1024), 16, 0, 0);                 \
    __builtin_amdgcn_global_load_lds((const unsigned*)(bsrc[0] + (size_t)kk * G1_BK * F_FF),       \
                                     (unsigned*)(lb + 8192 + ld * 1024), 16, 0, 0);                \
    if (ld < 2)                                                                                    \
      __builtin_amdgcn_global_load_lds((const unsigned*)(bsrc[1] + (size_t)kk * G1_BK * F_FF),     \
                                       (unsigned*)(lb + 8192 + (ld + 4) * 1024), 16, 0, 0);        \
  }
#pragma unroll
    for (int s = 0; s < G1_NST - 1; s++) G1_ISSUE(s, s);
    if (ld < 2) asm volatile("s_waitcnt vmcnt(12)" ::: "memory");
    else asm volatile("s_waitcnt vmcnt(9)" ::: "memory");
    __builtin_amdgcn_s_barrier();
    int bp = G1_NST - 1;
    for (int it = 0; it < G1_NK; it++) {
      if (ld < 2) asm volatile("s_waitcnt vmcnt(8)" ::: "memory");
      else asm volatile("s_waitcnt vmcnt(6)" ::: "memory");
      __builtin_amdgcn_s_barrier();
      int nx = it + G1_NST - 1;
      if (nx < G1_NK) { G1_ISSUE(nx, bp); }
      else asm volatile("s_waitcnt vmcnt(0)" ::: "memory");
      bp = bp + 1; if (bp >= G1_NST) bp = 0;
    }
    asm volatile("s_waitcnt vmcnt(0)" ::: "memory");
    __syncthreads();
  } else {
    const int wm = wave & 1, wn = wave >> 1;
    f32x4 acc[4][3];
#pragma unroll
    for (int i = 0; i < 4; i++)
#pragma unroll
      for (int j = 0; j < 3; j++) acc[i][j] = (f32x4){0.f, 0.f, 0.f, 0.f};
    const int q = lane >> 4, ii = lane & 15;
    const int aoff = (wm * 64 + ii) * 64 + ((q ^ ((0x78 >> (2 * ((ii >> 2) & 3))) & 3)) * 16);
    const int brow = (8 * q + (ii >> 2)) * 192 + 8 * (ii & 3);
#define G1_FRAGS(AF, BF, buf_)                                                                     \
  {                                                                                                \
    const char* lb = lbase + (buf_) * G1_STAGE_BYTES;                                              \
    _Pragma("unroll") for (int i = 0; i < 4; i++)                                                  \
      AF[i] = *reinterpret_cast<const bf16x8*>(lb + aoff + i * 1024);                              \
    _Pragma("unroll") for (int j = 0; j < 3; j++) {                                                \
      const char* p0 = lb + 8192 + brow + (((wn * 3 + j) ^ (q & 1)) * 32);                         \
      s16x4 lo = lds_tr16(reinterpret_cast<const u16*>(p0));                                       \
      s16x4 hi = lds_tr16(reinterpret_cast<const u16*>(p0 + 4 * 192));                             \
      BF[j] = (bf16x8){lo[0], lo[1], lo[2], lo[3], hi[0], hi[1], hi[2], hi[3]};                    \
    }                                                                                              \
  }
    bf16x8 af[4], bfr[3], an[4], bn[3];
    __builtin_amdgcn_s_barrier();
    STAMP1(h, 1);
    G1_FRAGS(af, bfr, 0);
    int b1 = 1;
    for (int it = 0; it < G1_NK; it++) {
      asm volatile("s_waitcnt lgkmcnt(0)" ::: "memory");
      __builtin_amdgcn_s_barrier();
      G1_FRAGS(an, bn, b1);
#pragma unroll
      for (int i = 0; i < 4; i++)
#pragma unroll
        for (int j = 0; j < 3; j++)
          acc[i][j] = __builtin_amdgcn_mfma_f32_16x16x32_bf16(af[i], bfr[j], acc[i][j], 0, 0, 0);
#pragma unroll
      for (int i = 0; i < 4; i++) af[i] = an[i];
#pragma unroll
      for (int j = 0; j < 3; j++) bfr[j] = bn[j];
      b1 = b1 + 1; if (b1 >= G1_NST) b1 = 0;
    }
    STAMP1(h, 2);
    __syncthreads();
#pragma unroll
    for (int i = 0; i < 4; i++)
#pragma unroll
      for (int j = 0; j < 3; j++)
#pragma unroll
        for (int r = 0; r < 4; r++) {
          float v = rbf(acc[i][j][r]);
          float s = v / (1.f + __expf(-v));
          S.un.ot[wm * 64 + i * 16 + 4 * q + r][wn * 48 + j * 16 + ii] = f2bf(s);
        }
  }
  __syncthreads();
#pragma unroll
  for (int i = 0; i < 3; i++) {
    int c = tid + K1_THREADS * i;
    int r = c / 12, cc = c - r * 12;
    u32x4 v = *reinterpret_cast<const u32x4*>(&S.un.ot[r][cc * 8]);
    *reinterpret_cast<u32x4*>(h + (size_t)(m0 + r) * F_FF + n0 + cc * 8) = v;
  }
  STAMP1(h, 3);
}

__device__ void conv_block(int blk, const u16* __restrict__ u, const u16* __restrict__ logAre,
                           const u16* __restrict__ Aim, const u16* __restrict__ Cre,
                           const u16* __restrict__ Cim, const u16* __restrict__ logdt,
                           const u16* __restrict__ Dsk, u16* __restrict__ aout, char* smem) {
  ConvLds& S = *reinterpret_cast<ConvLds*>(smem);
  const int tid = threadIdx.x;
  const int lane = tid & 63;
  const int wave = tid >> 6;
  const int cg = wave >> 2, w4 = wave & 3, gt = tid & 255;
  const int d0 = 2 * blk;
  char* dbase = (char*)aout - (8u << 20);
  STAMP1(dbase, 0);

  unsigned ureg[4];
#pragma unroll
  for (int i = 0; i < 4; i++)
    ureg[i] = *reinterpret_cast<const unsigned*>(u + (size_t)(tid + K1_THREADS * i) * D_CH + d0);
  if (tid < 256) {
    const int ch = tid >> 7, n = tid & 127, d = d0 + ch;
    const float dt = rbf(expf(bf2f(logdt[d])));
    float are = -rbf(expf(bf2f(logAre[d * N_ST + n])));
    float aim = bf2f(Aim[d * N_ST + n]);
    float cre = bf2f(Cre[d * N_ST + n]) * dt;
    float cim = bf2f(Cim[d * N_ST + n]) * dt;
    double al = (double)(are * dt) * 1.4426950408889634;
    double be = (double)(aim * dt) * 0.15915494309189535;
    float beh = (float)be;
    float bel = (float)(be - (double)beh);
    float mag2 = cre * cre + cim * cim;
    float lnc = 0.5f * log2f(mag2) + 1.0f;
    float ph = atan2f(cim, cre) * 0.15915494309189535f;
    S.prmA[ch][n] = (f32x4){(float)al, beh, bel, lnc};
    S.prmB[ch][n] = ph;
  }
  __syncthreads();

  STAMP1(dbase, 1);
  const int r16 = lane & 15, q = lane >> 4;
  {
    const int pass = 0;
    const int ch = cg;
    {
      float pr[3];
      pr[0] = (float)r16;
      pr[1] = (r16 < 8) ? (float)(256 + 2 * r16) : (float)(512 + 4 * (r16 & 3) + 256 * ((r16 - 8) >> 2));
      pr[2] = (r16 < 8) ? (float)(1024 + 8 * (r16 & 1) + 256 * (r16 >> 1)) : 2048.f;
      const float pc = (float)(16 * r16);
      f32x4 acc[3];
#pragma unroll
      for (int i = 0; i < 3; i++) acc[i] = (f32x4){0.f, 0.f, 0.f, 0.f};
#pragma unroll
      for (int s = 0; s < 8; s++) {
        int n = 32 * w4 + 4 * s + q;
        f32x4 pv = S.prmA[ch][n];
        float ph = S.prmB[ch][n];
        float al = pv[0], beh = pv[1], bel = pv[2], lnc = pv[3];
        float bre, bim;
        {
          float t = __builtin_amdgcn_fractf(beh * pc);
          t = fmaf(bel, pc, t);
          float mg = __builtin_amdgcn_exp2f(al * pc);
          bre = mg * __builtin_amdgcn_cosf(t);
          bim = mg * __builtin_amdgcn_sinf(t);
        }
#pragma unroll
        for (int i = 0; i < 3; i++) {
          float t = __builtin_amdgcn_fractf(beh * pr[i]);
          t = fmaf(bel, pr[i], t + ph);
          float mg = __builtin_amdgcn_exp2f(fmaf(al, pr[i], lnc));
          float are_ = mg * __builtin_amdgcn_cosf(t);
          float ain_ = -mg * __builtin_amdgcn_sinf(t);
          acc[i] = __builtin_amdgcn_mfma_f32_16x16x4f32(are_, bre, acc[i], 0, 0, 0);
          acc[i] = __builtin_amdgcn_mfma_f32_16x16x4f32(ain_, bim, acc[i], 0, 0, 0);
        }
      }
#pragma unroll
      for (int i = 0; i < 3; i++)
#pragma unroll
        for (int j = 0; j < 4; j++) S.un.kp[cg][w4][(16 * i + 4 * q + j) * 16 + r16] = acc[i][j];
    }
    if (pass == 0) {
      STAMP1(dbase, 6);
      S.us[tid >> 8][tid & 255] = 0;
#pragma unroll
      for (int i = 0; i < 4; i++) {
        int l = 256 + tid + K1_THREADS * i;
        S.us[0][l] = (u16)(ureg[i] & 0xffff);
        S.us[1][l] = (u16)(ureg[i] >> 16);
      }
    }
    __syncthreads();
    if (pass == 0) STAMP1(dbase, 2);
#pragma unroll
    for (int i = 0; i < 3; i++) {
      int e = gt + 256 * i;
      S.ksum[cg][e] = (S.un.kp[cg][0][e] + S.un.kp[cg][1][e]) + (S.un.kp[cg][2][e] + S.un.kp[cg][3][e]);
    }
    __syncthreads();
#pragma unroll
    for (int it = 0; it < 5; it++) {
      const int x2 = gt + 256 * it;
      if (it < 4 || x2 < CV_R_ELEMS / 2) {
        unsigned hv[3], lv[3];
#pragma unroll
        for (int e = 0; e < 3; e++) {
          int m = 2047 - (2 * x2 - 1 + e);
          bool ok = (unsigned)m <= 2047u;
          int p = (int)rbf((float)(ok ? m : 0));
          int sh = max(0, 24 - (int)__clz(p));
          int rowbase = sh ? 8 + 8 * sh : 0;
          int row = rowbase + ((p & 15) >> sh) + (16 >> sh) * (max(p - (128 << sh), 0) >> 8);
          float kv = S.ksum[cg][row * 16 + ((p >> 4) & 15)];
          kv = ok ? kv : 0.f;
          u16 hi = f2bf(kv);
          hv[e] = hi;
          lv[e] = f2bf(kv - bf2f(hi));
        }
        reinterpret_cast<unsigned*>(S.un.r[cg][0])[x2] = hv[1] | (hv[2] << 16);
        reinterpret_cast<unsigned*>(S.un.r[cg][2])[x2] = lv[1] | (lv[2] << 16);
        reinterpret_cast<unsigned*>(S.un.r[cg][1])[x2] = hv[0] | (hv[1] << 16);
        reinterpret_cast<unsigned*>(S.un.r[cg][3])[x2] = lv[0] | (lv[1] << 16);
      }
    }
    __syncthreads();

    if (pass == 0) STAMP1(dbase, 3);
    const u16* us = S.us[ch];
    f32x4 acc[8];
#pragma unroll
    for (int t = 0; t < 8; t++) acc[t] = (f32x4){0.f, 0.f, 0.f, 0.f};
    for (int kk = 0; kk < 2; kk++) {
      const int kp_ = 4 * w4 + 2 * kk;
      bf16x8 bfr[8];
#pragma unroll
      for (int dl = 0; dl < 8; dl++)
        bfr[dl] = *reinterpret_cast<const bf16x8*>(&us[256 + 16 * (16 * dl - kp_ + r16 - 1) + 8 * q]);
      const int xx = 2047 - (16 * (kp_ + 1) + r16 - 8 * q);
#pragma unroll
      for (int part = 0; part < 2; part++) {
        const unsigned* rp = (xx & 1) ? reinterpret_cast<const unsigned*>(&S.un.r[cg][2 * part + 1][xx + 1])
                                      : reinterpret_cast<const unsigned*>(&S.un.r[cg][2 * part][xx]);
        u32x4 af[8];
#pragma unroll
        for (int kb = 0; kb < 8; kb++)
          af[kb] = (u32x4){rp[0 - 128 * kb], rp[1 - 128 * kb], rp[2 - 128 * kb], rp[3 - 128 * kb]};
#pragma unroll
        for (int kb = 0; kb < 8; kb++)
#pragma unroll
          for (int t = kb; t < 8; t++)
            acc[t] = __builtin_amdgcn_mfma_f32_16x16x32_bf16(__builtin_bit_cast(bf16x8, af[kb]), bfr[t - kb], acc[t], 0, 0, 0);
      }
    }
    __syncthreads();
    f32x4* red = reinterpret_cast<f32x4*>(&S.un.r[cg][0][0]);
    {
      const bool hi_half = (w4 >> 1) != 0;
      f32x4 s0, s1, s2, s3, k0, k1_, k2_, k3;
      if (hi_half) { s0 = acc[0]; s1 = acc[1]; s2 = acc[2]; s3 = acc[3]; k0 = acc[4]; k1_ = acc[5]; k2_ = acc[6]; k3 = acc[7]; }
      else { s0 = acc[4]; s1 = acc[5]; s2 = acc[6]; s3 = acc[7]; k0 = acc[0]; k1_ = acc[1]; k2_ = acc[2]; k3 = acc[3]; }
      red[(w4 * 4 + 0) * 64 + lane] = s0;
      red[(w4 * 4 + 1) * 64 + lane] = s1;
      red[(w4 * 4 + 2) * 64 + lane] = s2;
      red[(w4 * 4 + 3) * 64 + lane] = s3;
      __syncthreads();
      k0 += red[((w4 ^ 2) * 4 + 0) * 64 + lane];
      k1_ += red[((w4 ^ 2) * 4 + 1) * 64 + lane];
      k2_ += red[((w4 ^ 2) * 4 + 2) * 64 + lane];
      k3 += red[((w4 ^ 2) * 4 + 3) * 64 + lane];
      __syncthreads();
      const bool od = (w4 & 1) != 0;
      f32x4 t0, t1, m0_, m1_;
      if (od) { t0 = k0; t1 = k1_; m0_ = k2_; m1_ = k3; }
      else { t0 = k2_; t1 = k3; m0_ = k0; m1_ = k1_; }
      red[(w4 * 2 + 0) * 64 + lane] = t0;
      red[(w4 * 2 + 1) * 64 + lane] = t1;
      __syncthreads();
      m0_ += red[((w4 ^ 1) * 2 + 0) * 64 + lane];
      m1_ += red[((w4 ^ 1) * 2 + 1) * 64 + lane];
      const float dsk = bf2f(Dsk[d0 + ch]);
      const int tb = 2 * w4;
      {
        int l = 16 * (16 * tb + r16) + 4 * q;
        s16x4 ov;
#pragma unroll
        for (int r = 0; r < 4; r++) ov[r] = (short)f2bf(rbf(m0_[r]) + rbf(bf2f(us[256 + l + r]) * dsk));
        *reinterpret_cast<s16x4*>(aout + (size_t)(d0 + ch) * L_SEQ + l) = ov;
        l += 256;
#pragma unroll
        for (int r = 0; r < 4; r++) ov[r] = (short)f2bf(rbf(m1_[r]) + rbf(bf2f(us[256 + l + r]) * dsk));
        *reinterpret_cast<s16x4*>(aout + (size_t)(d0 + ch) * L_SEQ + l) = ov;
      }
    }
    __syncthreads();
    STAMP1(dbase, 4 + pass);
  }
}

__device__ void gate_block(int bid, const u16* __restrict__ x, const u16* __restrict__ gw,
                           float* __restrict__ g) {
  const int lane = threadIdx.x & 63;
  const int wave = threadIdx.x >> 6;
  const unsigned* gw32 = reinterpret_cast<const unsigned*>(gw);
  unsigned gwv[5], xv[4][5];
#pragma unroll
  for (int i = 0; i < 5; i++) gwv[i] = gw32[lane + 64 * i];
#pragma unroll
  for (int rr = 0; rr < 4; rr++) {
    int row = bid * 32 + wave * 4 + rr;
    const unsigned* xr = reinterpret_cast<const unsigned*>(x + (size_t)row * D_CH);
#pragma unroll
    for (int i = 0; i < 5; i++) xv[rr][i] = xr[lane + 64 * i];
  }
#pragma unroll
  for (int rr = 0; rr < 4; rr++) {
    int row = bid * 32 + wave * 4 + rr;
    float s = 0.f;
#pragma unroll
    for (int i = 0; i < 5; i++) {
      s += rbf(bf2f((u16)(xv[rr][i] & 0xffff)) * bf2f((u16)(gwv[i] & 0xffff)));
      s += rbf(bf2f((u16)(xv[rr][i] >> 16)) * bf2f((u16)(gwv[i] >> 16)));
    }
#pragma unroll
    for (int off = 32; off > 0; off >>= 1) s += __shfl_xor(s, off);
    if (lane == 0) {
      float sv = rbf(s);
      g[row] = rbf(1.f / (1.f + __expf(-sv)));
    }
  }
}

__global__ void __launch_bounds__(K1_THREADS, 4) k1(const u16* __restrict__ x, const u16* __restrict__ logAre,
                                          const u16* __restrict__ Aim, const u16* __restrict__ Cre,
                                          const u16* __restrict__ Cim, const u16* __restrict__ logdt,
                                          const u16* __restrict__ Dsk, const u16* __restrict__ w1,
                                          const u16* __restrict__ gw, u16* __restrict__ h,
                                          u16* __restrict__ aout, float* __restrict__ g) {
  __shared__ __align__(16) char smem[sizeof(ConvLds) > sizeof(G1Lds) ? sizeof(ConvLds) : sizeof(G1Lds)];
  int bid = blockIdx.x;
  if (bid < CONV_BLOCKS) {
    conv_block(bid, x, logAre, Aim, Cre, Cim, logdt, Dsk, aout, smem);
  } else if (bid < CONV_BLOCKS + G1_BLOCKS) {
    gemm1_block(bid - CONV_BLOCKS, x, w1, h, smem);
  } else {
    gate_block(bid - CONV_BLOCKS - G1_BLOCKS, x, gw, g);
  }
}

#define G2_BM 64
#define G2_BN 80
#define G2_BK 64
#define G2_TILES_N (D_CH / G2_BN)
#define G2_TILES_M (L_SEQ / G2_BM)
#define G2_NST 7
#define G2_NK (F_FF / G2_BK)
#define G2_OLD 88

#define G2_STAGE_BYTES (8192 + 10240)
struct __align__(16) G2Lds {
  union {
    u16 st[G2_NST][G2_STAGE_BYTES / 2];
    struct {
      float red[2][2][5][64][4];
      u16 ot[G2_BM][G2_OLD];
    } e;
  } un;
};

__global__ void __launch_bounds__(384) k2(const u16* __restrict__ h, const u16* __restrict__ w2,
                                          const u16* __restrict__ aout, const float* __restrict__ g,
                                          u16* __restrict__ out) {
  __shared__ G2Lds S;
  const int tid = threadIdx.x;
  const int lane = tid & 63;
  const int wave = tid >> 6;
  const int bid = blockIdx.x;
  const int xcd = bid & 7, loc = bid >> 3;
  const int tm = 4 * xcd + (loc >> 3), tn = loc & 7;
  const int m0 = tm * G2_BM, n0 = tn * G2_BN;
  const int rot = (3 * tn + 7 * (tm & 3)) % G2_NK;
  char* lbase = reinterpret_cast<char*>(&S.un.st[0][0]);
#define STAMP(i)

  if (wave >= 4) {
    const int ld = wave - 4;
    const u16* asrc;
    {
      int off = ld * 4096 + lane * 16;
      int r = off >> 7, pc = (off >> 4) & 7;
      int c = pc ^ (r & 7);
      asrc = h + (size_t)(m0 + r) * F_FF + c * 8;
    }
    const u16* bsrc[5];
#pragma unroll
    for (int i = 0; i < 5; i++) {
      int off = (5 * ld + i) * 1024 + lane * 16;
      int r = off / 160, c = (off % 160) >> 4;
      bsrc[i] = w2 + (size_t)r * D_CH + n0 + c * 8;
    }
#define G2_ISSUE(it_, buf_)                                                                        \
  {                                                                                                \
    int kk = (it_) + rot; if (kk >= G2_NK) kk -= G2_NK;                                            \
    char* lb = lbase + (buf_) * G2_STAGE_BYTES;                                                    \
    _Pragma("unroll") for (int i = 0; i < 4; i++)                                                  \
      __builtin_amdgcn_global_load_lds((const unsigned*)(asrc + (size_t)i * 8 * F_FF + kk * G2_BK), \
                                       (unsigned*)(lb + (4 * ld + i) * 1024), 16, 0, 0);           \
    _Pragma("unroll") for (int i = 0; i < 5; i++)                                                  \
      __builtin_amdgcn_global_load_lds((const unsigned*)(bsrc[i] + (size_t)kk * G2_BK * D_CH),     \
                                       (unsigned*)(lb + 8192 + (5 * ld + i) * 1024), 16, 0, 0);    \
  }
#pragma unroll
    for (int s = 0; s < G2_NST - 1; s++) G2_ISSUE(s, s);
    asm volatile("s_waitcnt vmcnt(45)" ::: "memory");
    __builtin_amdgcn_s_barrier();
    int bp = G2_NST - 1;
    for (int it = 0; it < G2_NK; it++) {
      asm volatile("s_waitcnt vmcnt(36)" ::: "memory");
      __builtin_amdgcn_s_barrier();
      int nx = it + G2_NST - 1;
      if (nx < G2_NK) { G2_ISSUE(nx, bp); }
      else asm volatile("s_waitcnt vmcnt(0)" ::: "memory");
      bp = bp + 1; if (bp >= G2_NST) bp = 0;
    }
    asm volatile("s_waitcnt vmcnt(0)" ::: "memory");
    __syncthreads();
    __syncthreads();
  } else {
    const int wm = wave & 1, wk = wave >> 1;
    f32x4 acc[2][5];
#pragma unroll
    for (int i = 0; i < 2; i++)
#pragma unroll
      for (int j = 0; j < 5; j++) acc[i][j] = (f32x4){0.f, 0.f, 0.f, 0.f};
    const int q = lane >> 4, ii = lane & 15;
    const int boff0 = 8192 + ((wk * 32 + 8 * q + (ii >> 2) + 4 * (q & 1)) * 80 + 4 * (ii & 3)) * 2;
    const int boff1 = 8192 + ((wk * 32 + 8 * q + (ii >> 2) + 4 * ((q & 1) ^ 1)) * 80 + 4 * (ii & 3)) * 2;
#define G2_FRAGS(AF, BF, buf_)                                                                     \
  {                                                                                                \
    const char* lb = lbase + (buf_) * G2_STAGE_BYTES;                                              \
    _Pragma("unroll") for (int i = 0; i < 2; i++) {                                                \
      int row = wm * 32 + i * 16 + (lane & 15);                                                    \
      int pc = (wk * 4 + (lane >> 4)) ^ (row & 7);                                                 \
      AF[i] = *reinterpret_cast<const bf16x8*>(lb + row * 128 + pc * 16);                          \
    }                                                                                              \
    _Pragma("unroll") for (int j = 0; j < 5; j++) {                                                \
      s16x4 x0 = lds_tr16(reinterpret_cast<const u16*>(lb + boff0 + j * 32));                      \
      s16x4 x1 = lds_tr16(reinterpret_cast<const u16*>(lb + boff1 + j * 32));                      \
      s16x4 lo = (q & 1) ? x1 : x0, hi = (q & 1) ? x0 : x1;                                        \
      BF[j] = (bf16x8){lo[0], lo[1], lo[2], lo[3], hi[0], hi[1], hi[2], hi[3]};                    \
    }                                                                                              \
  }
    s16x4 av4[5];
    f32x4 g4;
    const int row0 = m0 + wm * 32 + wk * 16 + 4 * q;
#pragma unroll
    for (int j = 0; j < 5; j++)
      av4[j] = *reinterpret_cast<const s16x4*>(aout + (size_t)(n0 + j * 16 + ii) * L_SEQ + row0);
    g4 = *reinterpret_cast<const f32x4*>(g + row0);

    bf16x8 af[2], bfr[5], an[2], bn[5];
    __builtin_amdgcn_s_barrier();
    STAMP(1);
    G2_FRAGS(af, bfr, 0);
    int b1 = 1;
    for (int it = 0; it < G2_NK; it++) {
      asm volatile("s_waitcnt lgkmcnt(0)" ::: "memory");
      __builtin_amdgcn_s_barrier();
      G2_FRAGS(an, bn, b1);
#pragma unroll
      for (int i = 0; i < 2; i++)
#pragma unroll
        for (int j = 0; j < 5; j++)
          acc[i][j] = __builtin_amdgcn_mfma_f32_16x16x32_bf16(af[i], bfr[j], acc[i][j], 0, 0, 0);
#pragma unroll
      for (int i = 0; i < 2; i++) af[i] = an[i];
#pragma unroll
      for (int j = 0; j < 5; j++) bfr[j] = bn[j];
      b1 = b1 + 1; if (b1 >= G2_NST) b1 = 0;
    }
    STAMP(2);
    __syncthreads();
#pragma unroll
    for (int j = 0; j < 5; j++)
      *reinterpret_cast<f32x4*>(&S.un.e.red[wm][wk][j][lane][0]) = wk ? acc[0][j] : acc[1][j];
    __syncthreads();
#pragma unroll
    for (int j = 0; j < 5; j++) {
      f32x4 o = *reinterpret_cast<const f32x4*>(&S.un.e.red[wm][wk ^ 1][j][lane][0]);
      f32x4 mine = wk ? acc[1][j] : acc[0][j];
#pragma unroll
      for (int r = 0; r < 4; r++) {
        float b = rbf(mine[r] + o[r]);
        float av = bf2f((u16)av4[j][r]);
        float t1 = rbf(g4[r] * av);
        float t2 = rbf(rbf(1.f - g4[r]) * b);
        S.un.e.ot[wm * 32 + wk * 16 + 4 * q + r][j * 16 + ii] = f2bf(t1 + t2);
      }
    }
  }
  __syncthreads();
  for (int c = tid; c < 640; c += 384) {
    int r = c / 10, cc = c - r * 10;
    u32x4 v = *reinterpret_cast<const u32x4*>(&S.un.e.ot[r][cc * 8]);
    *reinterpret_cast<u32x4*>(out + (size_t)(m0 + r) * D_CH + n0 + cc * 8) = v;
  }
  STAMP(3);
}

extern "C" void kernel_launch(void* const* d_in, const int* in_sizes, int n_in,
                              void* d_out, int out_size, void* d_ws, size_t ws_size,
                              hipStream_t stream) {
  const u16* u = (const u16*)d_in[0];
  const u16* logAre = (const u16*)d_in[1];
  const u16* Aim = (const u16*)d_in[2];
  const u16* Cre = (const u16*)d_in[3];
  const u16* Cim = (const u16*)d_in[4];
  const u16* logdt = (const u16*)d_in[5];
  const u16* Dsk = (const u16*)d_in[6];
  const u16* w1 = (const u16*)d_in[8];
  const u16* w2 = (const u16*)d_in[9];
  const u16* gw = (const u16*)d_in[10];
  u16* out = (u16*)d_out;
  char* ws = (char*)d_ws;
  u16* h = (u16*)ws;
  u16* aout = (u16*)(ws + (8u << 20));
  float* g = (float*)(ws + (12u << 20));

  k1<<<CONV_BLOCKS + G1_BLOCKS + GATE_BLOCKS, K1_THREADS, 0, stream>>>(u, logAre, Aim, Cre, Cim, logdt, Dsk,
                                                                w1, gw, h, aout, g);
  k2<<<G2_TILES_M * G2_TILES_N, 384, 0, stream>>>(h, w2, aout, g, out);
}
```

```cpp
#include <hip/hip_runtime.h>
#include <hip/hip_bf16.h>
#include <stdint.h>

#define L_SEQ 2048
#define D_CH 640
#define N_ST 128
#define F_FF 1728

typedef unsigned short u16;
#define STAMP1(base, i)
typedef __attribute__((ext_vector_type(8))) short bf16x8;
typedef __attribute__((ext_vector_type(4))) short s16x4;
typedef __attribute__((ext_vector_type(4))) float f32x4;
typedef __attribute__((ext_vector_type(4))) unsigned u32x4;

__device__ __forceinline__ float bf2f(u16 v) { return __uint_as_float(((unsigned)v) << 16); }
__device__ __forceinline__ u16 f2bf(float f) {
  unsigned u = __float_as_uint(f);
  u += 0x7FFFu + ((u >> 16) & 1u);
  return (u16)(u >> 16);
}
__device__ __forceinline__ float rbf(float f) { return bf2f(f2bf(f)); }

__device__ __forceinline__ s16x4 lds_tr16(const u16* p) {
  return __builtin_amdgcn_ds_read_tr16_b64_v4i16((__attribute__((address_space(3))) s16x4*)(p));
}

#define NTHR 512
#define G1_BM 128
#define G1_BN 192
#define G1_BK 64
#define G1_TILES_N (F_FF / G1_BN)
#define G1_BLOCKS 144
#define G1_NST 2
#define G1_NK (D_CH / G1_BK)
#define G1_OLD 200
#define CONV_BLOCKS (D_CH / 2)

typedef __attribute__((ext_vector_type(2))) __bf16 bf16v2;
typedef __attribute__((ext_vector_type(2))) float f32x2;
__device__ __forceinline__ unsigned pk_bf(float a, float b) {
  f32x2 v = {a, b};
  return __builtin_bit_cast(unsigned, __builtin_convertvector(v, bf16v2));
}
typedef unsigned long long u64;
__device__ __forceinline__ void st_coh64(void* p, u64 v) {
  __hip_atomic_store(reinterpret_cast<u64*>(p), v, __ATOMIC_RELAXED, __HIP_MEMORY_SCOPE_AGENT);
}
__device__ __forceinline__ u64 ld_coh64(const void* p) {
  return __hip_atomic_load(reinterpret_cast<const u64*>(p), __ATOMIC_RELAXED, __HIP_MEMORY_SCOPE_AGENT);
}
__device__ __forceinline__ u16 ld_coh16(const u16* p) {
  return __hip_atomic_load(p, __ATOMIC_RELAXED, __HIP_MEMORY_SCOPE_AGENT);
}
__device__ __forceinline__ float lo_f(unsigned w) { return __uint_as_float(w << 16); }
__device__ __forceinline__ float hi_f(unsigned w) { return __uint_as_float(w & 0xffff0000u); }

#define CV_US_ELEMS (256 + L_SEQ)
#define CV_R_ELEMS 2080

struct __align__(16) ConvLds {
  u16 us[2][CV_US_ELEMS];
  f32x4 prmA[2][N_ST];
  float prmB[2][N_ST];
  float ksum[2][768];
  union {
    float kp[2][4][768];
    u16 r[2][4][CV_R_ELEMS];
  } un;
};

#define G1_STAGE_BYTES (16384 + 24576)
struct __align__(16) G1Lds {
  union {
    u16 st[G1_NST][G1_STAGE_BYTES / 2];
    u16 ot[G1_BM][G1_OLD];
  } un;
};

__device__ __forceinline__ float silu_bf(float a) {
  float v = rbf(a);
  return v * __builtin_amdgcn_rcpf(1.f + __builtin_amdgcn_exp2f(v * -1.4426950408889634f));
}

__device__ __forceinline__ void gemm1_block(int bid, const u16* __restrict__ x, const u16* __restrict__ w1,
                            u16* __restrict__ h, char* smem) {
  G1Lds& S = *reinterpret_cast<G1Lds*>(smem);
  const int tid = threadIdx.x;
  const int lane = tid & 63;
  const int wave = tid >> 6;
  const int xcd = bid & 7, loc = bid >> 3;
  const int tm = 2 * xcd + loc / G1_TILES_N, tn = loc % G1_TILES_N;
  const int m0 = tm * G1_BM, n0 = tn * G1_BN;
  const int rot = (tn + 5 * (tm & 1)) % G1_NK;
  char* lbase = reinterpret_cast<char*>(&S.un.st[0][0]);
  const int dslot = 320 + bid; (void)dslot;
  STAMP1(h, 0);

  unsigned aofs, bofs[3];
  {
    int off = wave * 2048 + lane * 16;
    int r = off >> 7, pc = (off >> 4) & 7;
    aofs = (unsigned)((m0 + r) * D_CH + ((pc ^ (r & 7)) * 8));
#pragma unroll
    for (int i = 0; i < 3; i++) {
      int o2 = (3 * wave + i) * 1024 + lane * 16;
      int rb = o2 / 384, cp = (o2 % 384) >> 4;
      int c = cp ^ (2 * ((rb >> 1) & 1));
      bofs[i] = (unsigned)(rb * F_FF + n0 + c * 8);
    }
  }
#define G1_ISSUE(it_, buf_)                                                                        \
  {                                                                                                \
    int kk = (it_) + rot; if (kk >= G1_NK) kk -= G1_NK;                                            \
    char* lb = lbase + (buf_) * G1_STAGE_BYTES;                                                    \
    const u16* xa = x + kk * G1_BK;                                                                \
    const u16* wb = w1 + (size_t)kk * (G1_BK * F_FF);                                              \
    __builtin_amdgcn_global_load_lds((const unsigned*)(xa + aofs),                                 \
                                     (unsigned*)(lb + wave * 2048), 16, 0, 0);                     \
    __builtin_amdgcn_global_load_lds((const unsigned*)(xa + aofs + 8 * D_CH),                      \
                                     (unsigned*)(lb + wave * 2048 + 1024), 16, 0, 0);              \
    _Pragma("unroll") for (int i = 0; i < 3; i++)                                                  \
      __builtin_amdgcn_global_load_lds((const unsigned*)(wb + bofs[i]),                            \
                                       (unsigned*)(lb + 16384 + (3 * wave + i) * 1024), 16, 0, 0); \
  }
  G1_ISSUE(0, 0);

  const int wm = wave & 1, wn = wave >> 1;
  const int q = lane >> 4, ii = lane & 15;
  f32x4 acc[4][3];
#pragma unroll
  for (int i = 0; i < 4; i++)
#pragma unroll
    for (int j = 0; j < 3; j++) acc[i][j] = (f32x4){0.f, 0.f, 0.f, 0.f};
  const int aoff = (wm * 64 + ii) * 128 + ((q ^ (ii & 7)) * 16);
  int boff[3];
#pragma unroll
  for (int j = 0; j < 3; j++)
    boff[j] = 16384 + (8 * q + (ii >> 2)) * 384 + (((wn * 3 + j) ^ ((ii >> 3) & 1)) * 32) + 8 * (ii & 3);

  int bc = 0, bp = 1;
  for (int it = 0; it < G1_NK; it++) {
    asm volatile("s_waitcnt vmcnt(0)" ::: "memory");
    __builtin_amdgcn_s_barrier();
    asm volatile("" ::: "memory");
    if (it == 0) { STAMP1(h, 1); }
    if (it + 1 < G1_NK) { G1_ISSUE(it + 1, bp); }
    const char* lb = lbase + bc * G1_STAGE_BYTES;
#pragma unroll
    for (int hk = 0; hk < 2; hk++) {
      bf16x8 af[4], bfr[3];
#pragma unroll
      for (int i = 0; i < 4; i++)
        af[i] = *reinterpret_cast<const bf16x8*>(lb + (aoff ^ (hk * 64)) + i * 2048);
#pragma unroll
      for (int j = 0; j < 3; j++) {
        const char* p0 = lb + boff[j] + hk * (32 * 384);
        s16x4 lo = lds_tr16(reinterpret_cast<const u16*>(p0));
        s16x4 hi = lds_tr16(reinterpret_cast<const u16*>(p0 + 4 * 384));
        bfr[j] = (bf16x8){lo[0], lo[1], lo[2], lo[3], hi[0], hi[1], hi[2], hi[3]};
      }
#pragma unroll
      for (int i = 0; i < 4; i++)
#pragma unroll
        for (int j = 0; j < 3; j++)
          acc[i][j] = __builtin_amdgcn_mfma_f32_16x16x32_bf16(bfr[j], af[i], acc[i][j], 0, 0, 0);
    }
    bc = bc + 1; if (bc >= G1_NST) bc = 0;
    bp = bp + 1; if (bp >= G1_NST) bp = 0;
  }
  STAMP1(h, 2);
  __syncthreads();
#pragma unroll
  for (int i = 0; i < 4; i++)
#pragma unroll
    for (int j = 0; j < 3; j++) {
      uint2 o;
      o.x = pk_bf(silu_bf(acc[i][j][0]), silu_bf(acc[i][j][1]));
      o.y = pk_bf(silu_bf(acc[i][j][2]), silu_bf(acc[i][j][3]));
      *reinterpret_cast<uint2*>(&S.un.ot[wm * 64 + i * 16 + ii][wn * 48 + j * 16 + 4 * q]) = o;
    }
  __syncthreads();
#pragma unroll
  for (int i = 0; i < 6; i++) {
    int c = tid + NTHR * i;
    int r = c / 24, cc = c - r * 24;
    u32x4 v = *reinterpret_cast<const u32x4*>(&S.un.ot[r][cc * 8]);
    *reinterpret_cast<u32x4*>(h + (size_t)(m0 + r) * F_FF + n0 + cc * 8) = v;
  }
  STAMP1(h, 3);

}

#define SIDE_BLOCKS 48
__device__ __forceinline__ void side_block(int bid, const u16* __restrict__ x, const u16* __restrict__ gw,
                                           float* __restrict__ g, const u16* __restrict__ w2,
                                           u16* __restrict__ w2p) {
  const int tid = threadIdx.x;
  const int lane = tid & 63;
  const int wave = tid >> 6;
  u32x4 v[6];
#pragma unroll
  for (int i = 0; i < 6; i++)
    if (i < 5 || tid < 2880 - 5 * NTHR)
      v[i] = *reinterpret_cast<const u32x4*>(w2 + (size_t)(bid * 2880 + tid + NTHR * i) * 8);
  const unsigned* gw32 = reinterpret_cast<const unsigned*>(gw);
  unsigned gwv[5], xv[6][5];
#pragma unroll
  for (int i = 0; i < 5; i++) gwv[i] = gw32[lane + 64 * i];
#pragma unroll
  for (int rr = 0; rr < 6; rr++) {
    int row = bid * 43 + wave * 6 + rr;
    if (row > L_SEQ - 1) row = L_SEQ - 1;
    const unsigned* xr = reinterpret_cast<const unsigned*>(x + (size_t)row * D_CH);
#pragma unroll
    for (int i = 0; i < 5; i++) xv[rr][i] = xr[lane + 64 * i];
  }
#pragma unroll
  for (int i = 0; i < 6; i++) {
    int s = bid * 2880 + tid + NTHR * i;
    int row = s / 80, cc = s - row * 80;
    int t2 = cc / 10, c = cc - t2 * 10;
    int kk = row >> 6, r = row & 63;
    if (i < 5 || tid < 2880 - 5 * NTHR)
      *reinterpret_cast<u32x4*>(w2p + (size_t)(((t2 * 27 + kk) * 64 + r) * 80 + c * 8)) = v[i];
  }
#pragma unroll
  for (int rr = 0; rr < 6; rr++) {
    int row = bid * 43 + wave * 6 + rr;
    float s = 0.f;
#pragma unroll
    for (int i = 0; i < 5; i++) {
      s += rbf(lo_f(xv[rr][i]) * lo_f(gwv[i]));
      s += rbf(hi_f(xv[rr][i]) * hi_f(gwv[i]));
    }
#pragma unroll
    for (int off = 32; off > 0; off >>= 1) s += __shfl_xor(s, off);
    if (lane == 0 && row < L_SEQ && wave * 6 + rr < 43) {
      float sv = rbf(s);
      g[row] = rbf(1.f / (1.f + __expf(-sv)));
    }
  }
}

template <bool COH>
__device__ __forceinline__ void conv_block(int blk, const u16* __restrict__ u, const u16* __restrict__ logAre,
                           const u16* __restrict__ Aim, const u16* __restrict__ Cre,
                           const u16* __restrict__ Cim, const u16* __restrict__ logdt,
                           const u16* __restrict__ Dsk, u16* __restrict__ aout, char* smem) {
  ConvLds& S = *reinterpret_cast<ConvLds*>(smem);
  const int tid = threadIdx.x;
  const int lane = tid & 63;
  const int wave = tid >> 6;
  const int cg = wave >> 2, w4 = wave & 3, gt = tid & 255;
  const int d0 = 2 * blk;
  char* dbase = (char*)aout - (8u << 20);
  const int dslot = blk; (void)dslot; (void)dbase;
  STAMP1(dbase, 0);

  u16 p_ld = 0, p_la = 0, p_ai = 0, p_cr = 0, p_ci = 0;
  const int pch = (tid >> 7) & 1, pn = tid & 127;
  if (tid < 256) {
    const int d = d0 + pch;
    p_ld = logdt[d];
    p_la = logAre[d * N_ST + pn];
    p_ai = Aim[d * N_ST + pn];
    p_cr = Cre[d * N_ST + pn];
    p_ci = Cim[d * N_ST + pn];
  }
  unsigned ureg[4];
#pragma unroll
  for (int i = 0; i < 4; i++)
    ureg[i] = *reinterpret_cast<const unsigned*>(u + (size_t)(tid + NTHR * i) * D_CH + d0);
  if (tid < 256) {
    const float dt = rbf(expf(bf2f(p_ld)));
    float are = -rbf(expf(bf2f(p_la)));
    float aim = bf2f(p_ai);
    float cre = bf2f(p_cr) * dt;
    float cim = bf2f(p_ci) * dt;
    double al = (double)(are * dt) * 1.4426950408889634;
    double be = (double)(aim * dt) * 0.15915494309189535;
    float beh = __uint_as_float(__float_as_uint((float)be) & 0xfffff000u);
    float bel = (float)(be - (double)beh);
    float mx = fmaxf(fabsf(cre), fabsf(cim)), mn = fminf(fabsf(cre), fabsf(cim));
    float rt = mx > 0.f ? mn / mx : 0.f;
    float lnc = log2f(mx) + 0.5f * log2f(1.f + rt * rt) + 1.0f;
    float ph = atan2f(cim, cre) * 0.15915494309189535f;
    S.prmA[pch][pn] = (f32x4){(float)al, beh, bel, lnc};
    S.prmB[pch][pn] = ph;
  }
  __syncthreads();

  STAMP1(dbase, 1);
  const int r16 = lane & 15, q = lane >> 4;
  {
    const int pass = 0;
    const int ch = cg;
    {
      float pr[3];
      pr[0] = (float)r16;
      pr[1] = (r16 < 8) ? (float)(256 + 2 * r16) : (float)(512 + 4 * (r16 & 3) + 256 * ((r16 - 8) >> 2));
      pr[2] = (r16 < 8) ? (float)(1024 + 8 * (r16 & 1) + 256 * (r16 >> 1)) : 2048.f;
      const float pc = (float)(16 * r16);
      f32x4 acc[3];
#pragma unroll
      for (int i = 0; i < 3; i++) acc[i] = (f32x4){0.f, 0.f, 0.f, 0.f};
#pragma unroll
      for (int s = 0; s < 8; s++) {
        int n = 32 * w4 + 4 * s + q;
        f32x4 pv = S.prmA[ch][n];
        float ph = S.prmB[ch][n];
        float al = pv[0], beh = pv[1], bel = pv[2], lnc = pv[3];
        float bre, bim;
        {
          float t = __builtin_amdgcn_fractf(beh * pc);
          t = fmaf(bel, pc, t);
          float mg = __builtin_amdgcn_exp2f(al * pc);
          bre = mg * __builtin_amdgcn_cosf(t);
          bim = mg * __builtin_amdgcn_sinf(t);
        }
#pragma unroll
        for (int i = 0; i < 3; i++) {
          float t = __builtin_amdgcn_fractf(beh * pr[i]);
          t = fmaf(bel, pr[i], t + ph);
          float mg = __builtin_amdgcn_exp2f(fmaf(al, pr[i], lnc));
          float are_ = mg * __builtin_amdgcn_cosf(t);
          float ain_ = -mg * __builtin_amdgcn_sinf(t);
          acc[i] = __builtin_amdgcn_mfma_f32_16x16x4f32(are_, bre, acc[i], 0, 0, 0);
          acc[i] = __builtin_amdgcn_mfma_f32_16x16x4f32(ain_, bim, acc[i], 0, 0, 0);
        }
      }
#pragma unroll
      for (int i = 0; i < 3; i++)
#pragma unroll
        for (int j = 0; j < 4; j++) S.un.kp[cg][w4][(16 * i + 4 * q + j) * 16 + r16] = acc[i][j];
    }
    if (pass == 0) {
      STAMP1(dbase, 6);
      S.us[tid >> 8][tid & 255] = 0;
#pragma unroll
      for (int i = 0; i < 4; i++) {
        int l = 256 + tid + NTHR * i;
        S.us[0][l] = (u16)(ureg[i] & 0xffff);
        S.us[1][l] = (u16)(ureg[i] >> 16);
      }
    }
    __syncthreads();
    if (pass == 0) STAMP1(dbase, 2);
    {
      const int c = gt & 15, rr = gt >> 4;
      int idx[3];
      idx[0] = rr + 16 * c;
      idx[1] = (rr < 8) ? 256 + rr + 8 * c : 384 + (rr & 3) + 64 * ((rr - 8) >> 2) + 4 * c;
      idx[2] = (rr < 8) ? 512 + (rr & 1) + 32 * (rr >> 1) + 2 * c : (gt == 128 ? 640 : 641);
#pragma unroll
      for (int i = 0; i < 3; i++) {
        int e = gt + 256 * i;
        S.ksum[cg][idx[i]] = (S.un.kp[cg][0][e] + S.un.kp[cg][1][e]) + (S.un.kp[cg][2][e] + S.un.kp[cg][3][e]);
      }
    }
    __syncthreads();
    {
      const int sh = gt < 128 ? 3 : (gt < 192 ? 2 : (gt < 224 ? 1 : 0));
      const int add = sh ? (1 << (sh - 1)) - 1 : 0;
      const int lsb = sh ? 1 : 0;
      const float* tb = &S.ksum[cg][128 * sh];
      float kv[9];
#pragma unroll
      for (int e = 0; e < 9; e++) {
        int m = 2047 - 8 * gt - e;
        int mm = m < 0 ? 0 : m;
        kv[e] = tb[(mm + add + ((mm >> sh) & lsb)) >> sh];
      }
      kv[8] = (gt == 255) ? 0.f : kv[8];
      u32x4 h0, h1, l0, l1;
      float lo[9];
#pragma unroll
      for (int e = 0; e < 4; e++) {
        h0[e] = pk_bf(kv[2 * e], kv[2 * e + 1]);
        h1[e] = pk_bf(kv[2 * e + 1], kv[2 * e + 2]);
        lo[2 * e] = kv[2 * e] - lo_f(h0[e]);
        lo[2 * e + 1] = kv[2 * e + 1] - hi_f(h0[e]);
      }
      lo[8] = kv[8] - hi_f(h1[3]);
#pragma unroll
      for (int e = 0; e < 4; e++) {
        l0[e] = pk_bf(lo[2 * e], lo[2 * e + 1]);
        l1[e] = pk_bf(lo[2 * e + 1], lo[2 * e + 2]);
      }
      *reinterpret_cast<u32x4*>(&S.un.r[cg][0][8 * gt]) = h0;
      *reinterpret_cast<u32x4*>(&S.un.r[cg][1][8 * gt]) = h1;
      *reinterpret_cast<u32x4*>(&S.un.r[cg][2][8 * gt]) = l0;
      *reinterpret_cast<u32x4*>(&S.un.r[cg][3][8 * gt]) = l1;
      if (gt < 16) *reinterpret_cast<u32x4*>(&S.un.r[cg][gt >> 2][2048 + 8 * (gt & 3)]) = (u32x4){0u, 0u, 0u, 0u};
    }
    __syncthreads();

    if (pass == 0) STAMP1(dbase, 3);
    const u16* us = S.us[ch];
    f32x4 acc[8];
#pragma unroll
    for (int t = 0; t < 8; t++) acc[t] = (f32x4){0.f, 0.f, 0.f, 0.f};
    for (int kk = 0; kk < 2; kk++) {
      const int kp_ = 4 * w4 + 2 * kk;
      bf16x8 bfr[8];
#pragma unroll
      for (int dl = 0; dl < 8; dl++)
        bfr[dl] = *reinterpret_cast<const bf16x8*>(&us[256 + 16 * (16 * dl - kp_ + r16 - 1) + 8 * q]);
      const int xx = 2047 - (16 * (kp_ + 1) + r16 - 8 * q);
#pragma unroll
      for (int part = 0; part < 2; part++) {
        const unsigned* rp = (xx & 1) ? reinterpret_cast<const unsigned*>(&S.un.r[cg][2 * part + 1][xx - 1])
                                      : reinterpret_cast<const unsigned*>(&S.un.r[cg][2 * part][xx]);
        u32x4 af[8];
#pragma unroll
        for (int kb = 0; kb < 8; kb++)
          af[kb] = (u32x4){rp[0 - 128 * kb], rp[1 - 128 * kb], rp[2 - 128 * kb], rp[3 - 128 * kb]};
#pragma unroll
        for (int kb = 0; kb < 8; kb++)
#pragma unroll
          for (int t = kb; t < 8; t++)
            acc[t] = __builtin_amdgcn_mfma_f32_16x16x32_bf16(__builtin_bit_cast(bf16x8, af[kb]), bfr[t - kb], acc[t], 0, 0, 0);
      }
    }
    __syncthreads();
    f32x4* red = reinterpret_cast<f32x4*>(&S.un.r[cg][0][0]);
    {
      const bool hi_half = (w4 >> 1) != 0;
      f32x4 s0, s1, s2, s3, k0, k1_, k2_, k3;
      if (hi_half) { s0 = acc[0]; s1 = acc[1]; s2 = acc[2]; s3 = acc[3]; k0 = acc[4]; k1_ = acc[5]; k2_ = acc[6]; k3 = acc[7]; }
      else { s0 = acc[4]; s1 = acc[5]; s2 = acc[6]; s3 = acc[7]; k0 = acc[0]; k1_ = acc[1]; k2_ = acc[2]; k3 = acc[3]; }
      red[(w4 * 4 + 0) * 64 + lane] = s0;
      red[(w4 * 4 + 1) * 64 + lane] = s1;
      red[(w4 * 4 + 2) * 64 + lane] = s2;
      red[(w4 * 4 + 3) * 64 + lane] = s3;
      __syncthreads();
      k0 += red[((w4 ^ 2) * 4 + 0) * 64 + lane];
      k1_ += red[((w4 ^ 2) * 4 + 1) * 64 + lane];
      k2_ += red[((w4 ^ 2) * 4 + 2) * 64 + lane];
      k3 += red[((w4 ^ 2) * 4 + 3) * 64 + lane];
      __syncthreads();
      const bool od = (w4 & 1) != 0;
      f32x4 t0, t1, m0_, m1_;
      if (od) { t0 = k0; t1 = k1_; m0_ = k2_; m1_ = k3; }
      else { t0 = k2_; t1 = k3; m0_ = k0; m1_ = k1_; }
      red[(w4 * 2 + 0) * 64 + lane] = t0;
      red[(w4 * 2 + 1) * 64 + lane] = t1;
      __syncthreads();
      m0_ += red[((w4 ^ 1) * 2 + 0) * 64 + lane];
      m1_ += red[((w4 ^ 1) * 2 + 1) * 64 + lane];
      const float dsk = bf2f(Dsk[d0 + ch]);
      const int tb = 2 * w4;
      {
        int l = 16 * (16 * tb + r16) + 4 * q;
        s16x4 ov;
#pragma unroll
        for (int r = 0; r < 4; r++) ov[r] = (short)f2bf(rbf(m0_[r]) + rbf(bf2f(us[256 + l + r]) * dsk));
        if (COH) st_coh64(aout + (size_t)(d0 + ch) * L_SEQ + l, __builtin_bit_cast(u64, ov));
        else *reinterpret_cast<s16x4*>(aout + (size_t)(d0 + ch) * L_SEQ + l) = ov;
        l += 256;
#pragma unroll
        for (int r = 0; r < 4; r++) ov[r] = (short)f2bf(rbf(m1_[r]) + rbf(bf2f(us[256 + l + r]) * dsk));
        if (COH) st_coh64(aout + (size_t)(d0 + ch) * L_SEQ + l, __builtin_bit_cast(u64, ov));
        else *reinterpret_cast<s16x4*>(aout + (size_t)(d0 + ch) * L_SEQ + l) = ov;
      }
    }
    __syncthreads();
    STAMP1(dbase, 4 + pass);
  }
}

__global__ void __launch_bounds__(NTHR, 4) k1(const u16* __restrict__ x, const u16* __restrict__ logAre,
                                          const u16* __restrict__ Aim, const u16* __restrict__ Cre,
                                          const u16* __restrict__ Cim, const u16* __restrict__ logdt,
                                          const u16* __restrict__ Dsk, const u16* __restrict__ w1,
                                          const u16* __restrict__ gw, u16* __restrict__ h,
                                          u16* __restrict__ aout, float* __restrict__ g,
                                          const u16* __restrict__ w2, u16* __restrict__ w2p) {
  __shared__ __align__(16) char smem[sizeof(ConvLds) > sizeof(G1Lds) ? sizeof(ConvLds) : sizeof(G1Lds)];
  int bid = blockIdx.x;
  if (bid < CONV_BLOCKS) {
    conv_block<false>(bid, x, logAre, Aim, Cre, Cim, logdt, Dsk, aout, smem);
  } else if (bid < CONV_BLOCKS + G1_BLOCKS) {
    gemm1_block(bid - CONV_BLOCKS, x, w1, h, smem);
  } else {
    side_block(bid - CONV_BLOCKS - G1_BLOCKS, x, gw, g, w2, w2p);
  }
}

#define G2_BM 64
#define G2_BN 80
#define G2_BK 64
#define G2_TILES_N (D_CH / G2_BN)
#define G2_TILES_M (L_SEQ / G2_BM)
#define G2_NST 7
#define G2_NK (F_FF / G2_BK)
#define G2_OLD 88

#define G2_STAGE_BYTES (8192 + 10240)
struct __align__(16) G2Lds {
  union {
    u16 st[G2_NST][G2_STAGE_BYTES / 2];
    struct {
      float red[2][2][5][64][4];
      u16 ot[G2_BM][G2_OLD];
    } e;
  } un;
};

__global__ void __launch_bounds__(384) k2(const u16* __restrict__ h, const u16* __restrict__ w2,
                                          const u16* __restrict__ aout, const float* __restrict__ g,
                                          u16* __restrict__ out) {
  __shared__ G2Lds S;
  const int tid = threadIdx.x;
  const int lane = tid & 63;
  const int wave = tid >> 6;
  const int bid = blockIdx.x;
  const int xcd = bid & 7, loc = bid >> 3;
  const int tm = 4 * xcd + (loc >> 3), tn = loc & 7;
  const int m0 = tm * G2_BM, n0 = tn * G2_BN;
  const int rot = (3 * tn + 7 * (tm & 3)) % G2_NK;
  char* lbase = reinterpret_cast<char*>(&S.un.st[0][0]);
#define STAMP(i)

  if (wave >= 4) {
    const int ld = wave - 4;
    const u16* asrc;
    {
      int off = ld * 4096 + lane * 16;
      int r = off >> 7, pc = (off >> 4) & 7;
      int c = pc ^ (r & 7);
      asrc = h + (size_t)(m0 + r) * F_FF + c * 8;
    }
    const u16* bsrc = w2 + (size_t)tn * (G2_NK * 5120) + 5 * ld * 512 + lane * 8;
#define G2_ISSUE(it_, buf_)                                                                        \
  {                                                                                                \
    int kk = (it_) + rot; if (kk >= G2_NK) kk -= G2_NK;                                            \
    char* lb = lbase + (buf_) * G2_STAGE_BYTES;                                                    \
    _Pragma("unroll") for (int i = 0; i < 4; i++)                                                  \
      __builtin_amdgcn_global_load_lds((const unsigned*)(asrc + (size_t)i * 8 * F_FF + kk * G2_BK), \
                                       (unsigned*)(lb + (4 * ld + i) * 1024), 16, 0, 0);           \
    _Pragma("unroll") for (int i = 0; i < 5; i++)                                                  \
      __builtin_amdgcn_global_load_lds((const unsigned*)(bsrc + kk * 5120 + i * 512),              \
                                       (unsigned*)(lb + 8192 + (5 * ld + i) * 1024), 16, 0, 0);    \
  }
#pragma unroll
    for (int s = 0; s < G2_NST - 1; s++) G2_ISSUE(s, s);
    asm volatile("s_waitcnt vmcnt(45)" ::: "memory");
    __builtin_amdgcn_s_barrier();
    int bp = G2_NST - 1;
    for (int it = 0; it < G2_NK; it++) {
      asm volatile("s_waitcnt vmcnt(36)" ::: "memory");
      __builtin_amdgcn_s_barrier();
      int nx = it + G2_NST - 1;
      if (nx < G2_NK) { G2_ISSUE(nx, bp); }
      else asm volatile("s_waitcnt vmcnt(0)" ::: "memory");
      bp = bp + 1; if (bp >= G2_NST) bp = 0;
    }
    asm volatile("s_waitcnt vmcnt(0)" ::: "memory");
    __syncthreads();
    __syncthreads();
  } else {
    const int wm = wave & 1, wk = wave >> 1;
    f32x4 acc[2][5];
#pragma unroll
    for (int i = 0; i < 2; i++)
#pragma unroll
      for (int j = 0; j < 5; j++) acc[i][j] = (f32x4){0.f, 0.f, 0.f, 0.f};
    const int q = lane >> 4, ii = lane & 15;
    const int boff0 = 8192 + ((wk * 32 + 8 * q + (ii >> 2) + 4 * (q & 1)) * 80 + 4 * (ii & 3)) * 2;
    const int boff1 = 8192 + ((wk * 32 + 8 * q + (ii >> 2) + 4 * ((q & 1) ^ 1)) * 80 + 4 * (ii & 3)) * 2;
#define G2_FRAGS(AF, BF, buf_)                                                                     \
  {                                                                                                \
    const char* lb = lbase + (buf_) * G2_STAGE_BYTES;                                              \
    _Pragma("unroll") for (int i = 0; i < 2; i++) {                                                \
      int row = wm * 32 + i * 16 + (lane & 15);                                                    \
      int pc = (wk * 4 + (lane >> 4)) ^ (row & 7);                                                 \
      AF[i] = *reinterpret_cast<const bf16x8*>(lb + row * 128 + pc * 16);                          \
    }                                                                                              \
    _Pragma("unroll") for (int j = 0; j < 5; j++) {                                                \
      s16x4 x0 = lds_tr16(reinterpret_cast<const u16*>(lb + boff0 + j * 32));                      \
      s16x4 x1 = lds_tr16(reinterpret_cast<const u16*>(lb + boff1 + j * 32));                      \
      s16x4 lo = (q & 1) ? x1 : x0, hi = (q & 1) ? x0 : x1;                                        \
      BF[j] = (bf16x8){lo[0], lo[1], lo[2], lo[3], hi[0], hi[1], hi[2], hi[3]};                    \
    }                                                                                              \
  }
    s16x4 av4[5];
    f32x4 g4;
    const int row0 = m0 + wm * 32 + wk * 16 + 4 * q;
#pragma unroll
    for (int j = 0; j < 5; j++)
      av4[j] = *reinterpret_cast<const s16x4*>(aout + (size_t)(n0 + j * 16 + ii) * L_SEQ + row0);
    g4 = *reinterpret_cast<const f32x4*>(g + row0);

    bf16x8 af[2], bfr[5], an[2], bn[5];
    __builtin_amdgcn_s_barrier();
    STAMP(1);
    G2_FRAGS(af, bfr, 0);
    int b1 = 1;
    for (int it = 0; it < G2_NK; it++) {
      asm volatile("s_waitcnt lgkmcnt(0)" ::: "memory");
      __builtin_amdgcn_s_barrier();
      G2_FRAGS(an, bn, b1);
#pragma unroll
      for (int i = 0; i < 2; i++)
#pragma unroll
        for (int j = 0; j < 5; j++)
          acc[i][j] = __builtin_amdgcn_mfma_f32_16x16x32_bf16(af[i], bfr[j], acc[i][j], 0, 0, 0);
#pragma unroll
      for (int i = 0; i < 2; i++) af[i] = an[i];
#pragma unroll
      for (int j = 0; j < 5; j++) bfr[j] = bn[j];
      b1 = b1 + 1; if (b1 >= G2_NST) b1 = 0;
    }
    STAMP(2);
    __syncthreads();
#pragma unroll
    for (int j = 0; j < 5; j++)
      *reinterpret_cast<f32x4*>(&S.un.e.red[wm][wk][j][lane][0]) = wk ? acc[0][j] : acc[1][j];
    __syncthreads();
#pragma unroll
    for (int j = 0; j < 5; j++) {
      f32x4 o = *reinterpret_cast<const f32x4*>(&S.un.e.red[wm][wk ^ 1][j][lane][0]);
      f32x4 mine = wk ? acc[1][j] : acc[0][j];
#pragma unroll
      for (int r = 0; r < 4; r++) {
        float b = rbf(mine[r] + o[r]);
        float av = bf2f((u16)av4[j][r]);
        float t1 = rbf(g4[r] * av);
        float t2 = rbf(rbf(1.f - g4[r]) * b);
        S.un.e.ot[wm * 32 + wk * 16 + 4 * q + r][j * 16 + ii] = f2bf(t1 + t2);
      }
    }
  }
  __syncthreads();
  for (int c = tid; c < 640; c += 384) {
    int r = c / 10, cc = c - r * 10;
    u32x4 v = *reinterpret_cast<const u32x4*>(&S.un.e.ot[r][cc * 8]);
    *reinterpret_cast<u32x4*>(out + (size_t)(m0 + r) * D_CH + n0 + cc * 8) = v;
  }
  STAMP(3);
}

extern "C" void kernel_launch(void* const* d_in, const int* in_sizes, int n_in,
                              void* d_out, int out_size, void* d_ws, size_t ws_size,
                              hipStream_t stream) {
  const u16* u = (const u16*)d_in[0];
  const u16* logAre = (const u16*)d_in[1];
  const u16* Aim = (const u16*)d_in[2];
  const u16* Cre = (const u16*)d_in[3];
  const u16* Cim = (const u16*)d_in[4];
  const u16* logdt = (const u16*)d_in[5];
  const u16* Dsk = (const u16*)d_in[6];
  const u16* w1 = (const u16*)d_in[8];
  const u16* w2 = (const u16*)d_in[9];
  const u16* gw = (const u16*)d_in[10];
  u16* out = (u16*)d_out;
  char* ws = (char*)d_ws;
  u16* h = (u16*)ws;
  u16* aout = (u16*)(ws + (8u << 20));
  float* g = (float*)(ws + (12u << 20));
  u16* w2p = (u16*)(ws + (16u << 20));

  k1<<<CONV_BLOCKS + G1_BLOCKS + SIDE_BLOCKS, NTHR, 0, stream>>>(u, logAre, Aim, Cre, Cim, logdt, Dsk,
                                                                 w1, gw, h, aout, g, w2, w2p);
  k2<<<G2_TILES_M * G2_TILES_N, 384, 0, stream>>>(h, w2p, aout, g, out);
}
```

```cpp
#include <hip/hip_runtime.h>
#include <hip/hip_bf16.h>
#include <stdint.h>

#define EXP_A
#define EXP_B
#define WT_STORES
#define L_SEQ 2048
#define D_CH 640
#define N_ST 128
#define F_FF 1728

typedef unsigned short u16;
#define STAMP1(base, i)
typedef __attribute__((ext_vector_type(8))) short bf16x8;
typedef __attribute__((ext_vector_type(4))) short s16x4;
typedef __attribute__((ext_vector_type(4))) float f32x4;
typedef __attribute__((ext_vector_type(4))) unsigned u32x4;

__device__ __forceinline__ float bf2f(u16 v) { return __uint_as_float(((unsigned)v) << 16); }
typedef __attribute__((ext_vector_type(2))) __bf16 bf16v2;
typedef __attribute__((ext_vector_type(2))) float f32x2;
__device__ __forceinline__ unsigned pk_bf(float a, float b) {
  f32x2 v = {a, b};
  return __builtin_bit_cast(unsigned, __builtin_convertvector(v, bf16v2));
}
__device__ __forceinline__ u16 f2bf(float f) { return (u16)(pk_bf(f, f) & 0xffffu); }
__device__ __forceinline__ float rbf(float f) { return __uint_as_float(pk_bf(f, f) << 16); }

__device__ __forceinline__ s16x4 lds_tr16(const u16* p) {
  return __builtin_amdgcn_ds_read_tr16_b64_v4i16((__attribute__((address_space(3))) s16x4*)(p));
}

#define NTHR 512
#define G1_BM 128
#define G1_BN 192
#define G1_BK 64
#define G1_TILES_N (F_FF / G1_BN)
#define G1_BLOCKS 144
#define G1_NST 2
#define G1_NK (D_CH / G1_BK)
#define G1_OLD 200
#define CONV_BLOCKS (D_CH / 2)

typedef unsigned long long u64;
__device__ __forceinline__ void st_coh64(void* p, u64 v) {
  __hip_atomic_store(reinterpret_cast<u64*>(p), v, __ATOMIC_RELAXED, __HIP_MEMORY_SCOPE_AGENT);
}
__device__ __forceinline__ void st_wt128(void* p, u32x4 v) {
  asm volatile("global_store_dwordx4 %0, %1, off sc1\n\ts_nop 1" :: "v"(p), "v"(v) : "memory");
}
__device__ __forceinline__ u64 ld_coh64(const void* p) {
  return __hip_atomic_load(reinterpret_cast<const u64*>(p), __ATOMIC_RELAXED, __HIP_MEMORY_SCOPE_AGENT);
}
__device__ __forceinline__ u16 ld_coh16(const u16* p) {
  return __hip_atomic_load(p, __ATOMIC_RELAXED, __HIP_MEMORY_SCOPE_AGENT);
}
__device__ __forceinline__ float lo_f(unsigned w) { return __uint_as_float(w << 16); }
__device__ __forceinline__ float hi_f(unsigned w) { return __uint_as_float(w & 0xffff0000u); }

#define CV_US_ELEMS (256 + L_SEQ)
#define CV_R_ELEMS 2080

struct __align__(16) ConvLds {
  u16 us[2][CV_US_ELEMS];
  f32x4 prmA[2][N_ST];
  float prmB[2][N_ST];
  float ksum[2][768];
  union {
    float kp[2][4][768];
    u16 r[2][4][CV_R_ELEMS];
  } un;
};

#define G1_STAGE_BYTES (16384 + 24576)
struct __align__(16) G1Lds {
  union {
    u16 st[G1_NST][G1_STAGE_BYTES / 2];
    u16 ot[G1_BM][G1_OLD];
  } un;
};

__device__ __forceinline__ float silu_f(float v) {
  return v * __builtin_amdgcn_rcpf(1.f + __builtin_amdgcn_exp2f(v * -1.4426950408889634f));
}
__device__ __forceinline__ unsigned silu_pk(float a, float b) {
  unsigned w = pk_bf(a, b);
  return pk_bf(silu_f(lo_f(w)), silu_f(hi_f(w)));
}

__device__ __forceinline__ void gemm1_block(int bid, const u16* __restrict__ x, const u16* __restrict__ w1,
                            u16* __restrict__ h, char* smem) {
  G1Lds& S = *reinterpret_cast<G1Lds*>(smem);
  const int tid = threadIdx.x;
  const int lane = tid & 63;
  const int wave = tid >> 6;
  const int xcd = bid & 7, loc = bid >> 3;
  const int tm = 2 * xcd + loc / G1_TILES_N, tn = loc % G1_TILES_N;
  const int m0 = tm * G1_BM, n0 = tn * G1_BN;
  const int rot = (tn + 5 * (tm & 1)) % G1_NK;
  char* lbase = reinterpret_cast<char*>(&S.un.st[0][0]);
  const int dslot = 320 + bid; (void)dslot;
  STAMP1(h, 0);

  unsigned aofs, bofs[3];
  {
    int off = wave * 2048 + lane * 16;
    int r = off >> 7, pc = (off >> 4) & 7;
    aofs = (unsigned)((m0 + r) * D_CH + ((pc ^ (r & 7)) * 8));
#pragma unroll
    for (int i = 0; i < 3; i++) {
      int o2 = (3 * wave + i) * 1024 + lane * 16;
      int rb = o2 / 384, cp = (o2 % 384) >> 4;
      int c = cp ^ (2 * ((rb >> 1) & 1));
      bofs[i] = (unsigned)(rb * F_FF + n0 + c * 8);
    }
  }
#define G1_ISSUE(it_, buf_)                                                                        \
  {                                                                                                \
    int kk = (it_) + rot; if (kk >= G1_NK) kk -= G1_NK;                                            \
    char* lb = lbase + (buf_) * G1_STAGE_BYTES;                                                    \
    const u16* xa = x + kk * G1_BK;                                                                \
    const u16* wb = w1 + (size_t)kk * (G1_BK * F_FF);                                              \
    __builtin_amdgcn_global_load_lds((const unsigned*)(xa + aofs),                                 \
                                     (unsigned*)(lb + wave * 2048), 16, 0, 0);                     \
    __builtin_amdgcn_global_load_lds((const unsigned*)(xa + aofs + 8 * D_CH),                      \
                                     (unsigned*)(lb + wave * 2048 + 1024), 16, 0, 0);              \
    _Pragma("unroll") for (int i = 0; i < 3; i++)                                                  \
      __builtin_amdgcn_global_load_lds((const unsigned*)(wb + bofs[i]),                            \
                                       (unsigned*)(lb + 16384 + (3 * wave + i) * 1024), 16, 0, 0); \
  }
  G1_ISSUE(0, 0);

  const int wm = wave & 1, wn = wave >> 1;
  const int q = lane >> 4, ii = lane & 15;
  f32x4 acc[4][3];
#pragma unroll
  for (int i = 0; i < 4; i++)
#pragma unroll
    for (int j = 0; j < 3; j++) acc[i][j] = (f32x4){0.f, 0.f, 0.f, 0.f};
  const int aoff = (wm * 64 + ii) * 128 + ((q ^ (ii & 7)) * 16);
  int boff[3];
#pragma unroll
  for (int j = 0; j < 3; j++)
    boff[j] = 16384 + (8 * q + (ii >> 2)) * 384 + (((wn * 3 + j) ^ ((ii >> 3) & 1)) * 32) + 8 * (ii & 3);

  int bc = 0, bp = 1;
  for (int it = 0; it < G1_NK; it++) {
    asm volatile("s_waitcnt vmcnt(0) lgkmcnt(0)" ::: "memory");
    __builtin_amdgcn_s_barrier();
    asm volatile("" ::: "memory");
    if (it == 0) { STAMP1(h, 1); }
    if (it + 1 < G1_NK) { G1_ISSUE(it + 1, bp); }
    const char* lb = lbase + bc * G1_STAGE_BYTES;
#pragma unroll
    for (int hk = 0; hk < 2; hk++) {
      bf16x8 af[4], bfr[3];
#pragma unroll
      for (int i = 0; i < 4; i++)
        af[i] = *reinterpret_cast<const bf16x8*>(lb + (aoff ^ (hk * 64)) + i * 2048);
#pragma unroll
      for (int j = 0; j < 3; j++) {
        const char* p0 = lb + boff[j] + hk * (32 * 384);
        s16x4 lo = lds_tr16(reinterpret_cast<const u16*>(p0));
        s16x4 hi = lds_tr16(reinterpret_cast<const u16*>(p0 + 4 * 384));
        bfr[j] = (bf16x8){lo[0], lo[1], lo[2], lo[3], hi[0], hi[1], hi[2], hi[3]};
      }
#pragma unroll
      for (int i = 0; i < 4; i++)
#pragma unroll
        for (int j = 0; j < 3; j++)
          acc[i][j] = __builtin_amdgcn_mfma_f32_16x16x32_bf16(bfr[j], af[i], acc[i][j], 0, 0, 0);
    }
    bc = bc + 1; if (bc >= G1_NST) bc = 0;
    bp = bp + 1; if (bp >= G1_NST) bp = 0;
  }
  STAMP1(h, 2);
  __syncthreads();
#pragma unroll
  for (int i = 0; i < 4; i++)
#pragma unroll
    for (int j = 0; j < 3; j++) {
      uint2 o;
      o.x = silu_pk(acc[i][j][0], acc[i][j][1]);
      o.y = silu_pk(acc[i][j][2], acc[i][j][3]);
      *reinterpret_cast<uint2*>(&S.un.ot[wm * 64 + i * 16 + ii][wn * 48 + j * 16 + 4 * q]) = o;
    }
  __syncthreads();
#pragma unroll
  for (int i = 0; i < 6; i++) {
    int c = tid + NTHR * i;
    int r = c / 24, cc = c - r * 24;
    u32x4 v = *reinterpret_cast<const u32x4*>(&S.un.ot[r][cc * 8]);
    st_wt128(h + (size_t)(m0 + r) * F_FF + n0 + cc * 8, v);
  }
  STAMP1(h, 3);

}

#define SIDE_BLOCKS 48
__device__ __forceinline__ void side_block(int bid, const u16* __restrict__ x, const u16* __restrict__ gw,
                                           float* __restrict__ g, const u16* __restrict__ w2,
                                           u16* __restrict__ w2p) {
  const int tid = threadIdx.x;
  const int lane = tid & 63;
  const int wave = tid >> 6;
  u32x4 v[6];
#pragma unroll
  for (int i = 0; i < 6; i++)
    if (i < 5 || tid < 2880 - 5 * NTHR)
      v[i] = *reinterpret_cast<const u32x4*>(w2 + (size_t)(bid * 2880 + tid + NTHR * i) * 8);
  const unsigned* gw32 = reinterpret_cast<const unsigned*>(gw);
  unsigned gwv[5], xv[6][5];
#pragma unroll
  for (int i = 0; i < 5; i++) gwv[i] = gw32[lane + 64 * i];
#pragma unroll
  for (int rr = 0; rr < 6; rr++) {
    int row = bid * 43 + wave * 6 + rr;
    if (row > L_SEQ - 1) row = L_SEQ - 1;
    const unsigned* xr = reinterpret_cast<const unsigned*>(x + (size_t)row * D_CH);
#pragma unroll
    for (int i = 0; i < 5; i++) xv[rr][i] = xr[lane + 64 * i];
  }
#pragma unroll
  for (int i = 0; i < 6; i++) {
    int s = bid * 2880 + tid + NTHR * i;
    int row = s / 80, cc = s - row * 80;
    int t2 = cc / 10, c = cc - t2 * 10;
    int kk = row >> 6, r = row & 63;
    if (i < 5 || tid < 2880 - 5 * NTHR)
      st_wt128(w2p + (size_t)(((t2 * 27 + kk) * 64 + r) * 80 + c * 8), v[i]);
  }
#pragma unroll
  for (int rr = 0; rr < 6; rr++) {
    int row = bid * 43 + wave * 6 + rr;
    float s = 0.f;
#pragma unroll
    for (int i = 0; i < 5; i++) {
      s += rbf(lo_f(xv[rr][i]) * lo_f(gwv[i]));
      s += rbf(hi_f(xv[rr][i]) * hi_f(gwv[i]));
    }
#pragma unroll
    for (int off = 32; off > 0; off >>= 1) s += __shfl_xor(s, off);
    if (lane == 0 && row < L_SEQ && wave * 6 + rr < 43) {
      float sv = rbf(s);
      g[row] = rbf(1.f / (1.f + __expf(-sv)));
    }
  }
}

template <bool COH>
__device__ __forceinline__ void conv_block(int blk, const u16* __restrict__ u, const u16* __restrict__ logAre,
                           const u16* __restrict__ Aim, const u16* __restrict__ Cre,
                           const u16* __restrict__ Cim, const u16* __restrict__ logdt,
                           const u16* __restrict__ Dsk, u16* __restrict__ aout, char* smem) {
  ConvLds& S = *reinterpret_cast<ConvLds*>(smem);
  const int tid = threadIdx.x;
  const int lane = tid & 63;
  const int wave = tid >> 6;
  const int cg = wave >> 2, w4 = wave & 3, gt = tid & 255;
  const int d0 = 2 * ((blk & 7) * 40 + (blk >> 3));
  char* dbase = (char*)aout - (8u << 20);
  const int dslot = blk; (void)dslot; (void)dbase;
  STAMP1(dbase, 0);

  u16 p_ld = 0, p_la = 0, p_ai = 0, p_cr = 0, p_ci = 0;
  const int pch = (tid >> 7) & 1, pn = tid & 127;
  if (tid < 256) {
    const int d = d0 + pch;
    p_ld = logdt[d];
    p_la = logAre[d * N_ST + pn];
    p_ai = Aim[d * N_ST + pn];
    p_cr = Cre[d * N_ST + pn];
    p_ci = Cim[d * N_ST + pn];
  }
  unsigned ureg[4];
  if (tid < 256) {
    const float dt = rbf(expf(bf2f(p_ld)));
    float are = -rbf(expf(bf2f(p_la)));
    float aim = bf2f(p_ai);
    float cre = bf2f(p_cr) * dt;
    float cim = bf2f(p_ci) * dt;
    double al = (double)(are * dt) * 1.4426950408889634;
    double be = (double)(aim * dt) * 0.15915494309189535;
    float beh = __uint_as_float(__float_as_uint((float)be) & 0xfffff000u);
    float bel = (float)(be - (double)beh);
    float mx = fmaxf(fabsf(cre), fabsf(cim)), mn = fminf(fabsf(cre), fabsf(cim));
    float rt = mx > 0.f ? mn / mx : 0.f;
    float lnc = log2f(mx) + 0.5f * log2f(1.f + rt * rt) + 1.0f;
    float ph = atan2f(cim, cre) * 0.15915494309189535f;
    S.prmA[pch][pn] = (f32x4){(float)al, beh, bel, lnc};
    S.prmB[pch][pn] = ph;
  }
  __syncthreads();
  {
    int uo = tid * D_CH + d0;
    asm volatile("" : "+v"(uo) :: "memory");
#pragma unroll
    for (int i = 0; i < 4; i++)
      ureg[i] = *reinterpret_cast<const unsigned*>(u + uo + NTHR * i * D_CH);
  }

  STAMP1(dbase, 1);
  const int r16 = lane & 15, q = lane >> 4;
  {
    const int pass = 0;
    const int ch = cg;
    {
      float pr[3];
      pr[0] = (float)r16;
      pr[1] = (r16 < 8) ? (float)(256 + 2 * r16) : (float)(512 + 4 * (r16 & 3) + 256 * ((r16 - 8) >> 2));
      pr[2] = (r16 < 8) ? (float)(1024 + 8 * (r16 & 1) + 256 * (r16 >> 1)) : 2048.f;
      const float pc = (float)(16 * r16);
      f32x4 acc[3];
#pragma unroll
      for (int i = 0; i < 3; i++) acc[i] = (f32x4){0.f, 0.f, 0.f, 0.f};
#pragma unroll
      for (int s = 0; s < 8; s++) {
        int n = 32 * w4 + 4 * s + q;
        f32x4 pv = S.prmA[ch][n];
        float ph = S.prmB[ch][n];
        float al = pv[0], beh = pv[1], bel = pv[2], lnc = pv[3];
        float bre, bim;
        {
          float t = __builtin_amdgcn_fractf(beh * pc);
          t = fmaf(bel, pc, t);
          float mg = __builtin_amdgcn_exp2f(al * pc);
          bre = mg * __builtin_amdgcn_cosf(t);
          bim = mg * __builtin_amdgcn_sinf(t);
        }
#pragma unroll
        for (int i = 0; i < 3; i++) {
          float t = __builtin_amdgcn_fractf(beh * pr[i]);
          t = fmaf(bel, pr[i], t + ph);
          float mg = __builtin_amdgcn_exp2f(fmaf(al, pr[i], lnc));
          float are_ = mg * __builtin_amdgcn_cosf(t);
          float ain_ = -mg * __builtin_amdgcn_sinf(t);
          acc[i] = __builtin_amdgcn_mfma_f32_16x16x4f32(are_, bre, acc[i], 0, 0, 0);
          acc[i] = __builtin_amdgcn_mfma_f32_16x16x4f32(ain_, bim, acc[i], 0, 0, 0);
        }
      }
#pragma unroll
      for (int i = 0; i < 3; i++)
#pragma unroll
        for (int j = 0; j < 4; j++) S.un.kp[cg][w4][(16 * i + 4 * q + j) * 16 + r16] = acc[i][j];
    }
    if (pass == 0) {
      STAMP1(dbase, 6);
      S.us[tid >> 8][tid & 255] = 0;
#pragma unroll
      for (int i = 0; i < 4; i++) {
        int l = 256 + tid + NTHR * i;
        S.us[0][l] = (u16)(ureg[i] & 0xffff);
        S.us[1][l] = (u16)(ureg[i] >> 16);
      }
    }
    __syncthreads();
    if (pass == 0) STAMP1(dbase, 2);
    {
      const int c = gt & 15, rr = gt >> 4;
      int idx[3];
      idx[0] = rr + 16 * c;
      idx[1] = (rr < 8) ? 256 + rr + 8 * c : 384 + (rr & 3) + 64 * ((rr - 8) >> 2) + 4 * c;
      idx[2] = (rr < 8) ? 512 + (rr & 1) + 32 * (rr >> 1) + 2 * c : (gt == 128 ? 640 : 641);
#pragma unroll
      for (int i = 0; i < 3; i++) {
        int e = gt + 256 * i;
        S.ksum[cg][idx[i]] = (S.un.kp[cg][0][e] + S.un.kp[cg][1][e]) + (S.un.kp[cg][2][e] + S.un.kp[cg][3][e]);
      }
    }
    __syncthreads();
    {
      const int sh = gt < 128 ? 3 : (gt < 192 ? 2 : (gt < 224 ? 1 : 0));
      const int add = sh ? (1 << (sh - 1)) - 1 : 0;
      const int lsb = sh ? 1 : 0;
      const float* tb = &S.ksum[cg][128 * sh];
      float kv[9];
#pragma unroll
      for (int e = 0; e < 9; e++) {
        int m = 2047 - 8 * gt - e;
        int mm = m < 0 ? 0 : m;
        kv[e] = tb[(mm + add + ((mm >> sh) & lsb)) >> sh];
      }
      kv[8] = (gt == 255) ? 0.f : kv[8];
      u32x4 h0, h1, l0, l1;
      float lo[9];
#pragma unroll
      for (int e = 0; e < 4; e++) {
        h0[e] = pk_bf(kv[2 * e], kv[2 * e + 1]);
        h1[e] = pk_bf(kv[2 * e + 1], kv[2 * e + 2]);
        lo[2 * e] = kv[2 * e] - lo_f(h0[e]);
        lo[2 * e + 1] = kv[2 * e + 1] - hi_f(h0[e]);
      }
      lo[8] = kv[8] - hi_f(h1[3]);
#pragma unroll
      for (int e = 0; e < 4; e++) {
        l0[e] = pk_bf(lo[2 * e], lo[2 * e + 1]);
        l1[e] = pk_bf(lo[2 * e + 1], lo[2 * e + 2]);
      }
      *reinterpret_cast<u32x4*>(&S.un.r[cg][0][8 * gt]) = h0;
      *reinterpret_cast<u32x4*>(&S.un.r[cg][1][8 * gt]) = h1;
      *reinterpret_cast<u32x4*>(&S.un.r[cg][2][8 * gt]) = l0;
      *reinterpret_cast<u32x4*>(&S.un.r[cg][3][8 * gt]) = l1;
      if (gt < 16) *reinterpret_cast<u32x4*>(&S.un.r[cg][gt >> 2][2048 + 8 * (gt & 3)]) = (u32x4){0u, 0u, 0u, 0u};
    }
    __syncthreads();

    if (pass == 0) STAMP1(dbase, 3);
    const u16* us = S.us[ch];
    f32x4 acc[8];
#pragma unroll
    for (int t = 0; t < 8; t++) acc[t] = (f32x4){0.f, 0.f, 0.f, 0.f};
    for (int kk = 0; kk < 2; kk++) {
      const int kp_ = 4 * w4 + 2 * kk;
      bf16x8 bfr[8];
#pragma unroll
      for (int dl = 0; dl < 8; dl++)
        bfr[dl] = *reinterpret_cast<const bf16x8*>(&us[256 + 16 * (16 * dl - kp_ + r16 - 1) + 8 * q]);
      const int xx = 2047 - (16 * (kp_ + 1) + r16 - 8 * q);
#pragma unroll
      for (int part = 0; part < 2; part++) {
        const unsigned* rp = (xx & 1) ? reinterpret_cast<const unsigned*>(&S.un.r[cg][2 * part + 1][xx - 1])
                                      : reinterpret_cast<const unsigned*>(&S.un.r[cg][2 * part][xx]);
        u32x4 af[8];
#pragma unroll
        for (int kb = 0; kb < 8; kb++)
          af[kb] = (u32x4){rp[0 - 128 * kb], rp[1 - 128 * kb], rp[2 - 128 * kb], rp[3 - 128 * kb]};
#pragma unroll
        for (int kb = 0; kb < 8; kb++)
#pragma unroll
          for (int t = kb; t < 8; t++)
            acc[t] = __builtin_amdgcn_mfma_f32_16x16x32_bf16(__builtin_bit_cast(bf16x8, af[kb]), bfr[t - kb], acc[t], 0, 0, 0);
      }
    }
    __syncthreads();
    f32x4* red = reinterpret_cast<f32x4*>(&S.un.r[cg][0][0]);
    {
      const bool hi_half = (w4 >> 1) != 0;
      f32x4 s0, s1, s2, s3, k0, k1_, k2_, k3;
      if (hi_half) { s0 = acc[0]; s1 = acc[1]; s2 = acc[2]; s3 = acc[3]; k0 = acc[4]; k1_ = acc[5]; k2_ = acc[6]; k3 = acc[7]; }
      else { s0 = acc[4]; s1 = acc[5]; s2 = acc[6]; s3 = acc[7]; k0 = acc[0]; k1_ = acc[1]; k2_ = acc[2]; k3 = acc[3]; }
      red[(w4 * 4 + 0) * 64 + lane] = s0;
      red[(w4 * 4 + 1) * 64 + lane] = s1;
      red[(w4 * 4 + 2) * 64 + lane] = s2;
      red[(w4 * 4 + 3) * 64 + lane] = s3;
      __syncthreads();
      k0 += red[((w4 ^ 2) * 4 + 0) * 64 + lane];
      k1_ += red[((w4 ^ 2) * 4 + 1) * 64 + lane];
      k2_ += red[((w4 ^ 2) * 4 + 2) * 64 + lane];
      k3 += red[((w4 ^ 2) * 4 + 3) * 64 + lane];
      __syncthreads();
      const bool od = (w4 & 1) != 0;
      f32x4 t0, t1, m0_, m1_;
      if (od) { t0 = k0; t1 = k1_; m0_ = k2_; m1_ = k3; }
      else { t0 = k2_; t1 = k3; m0_ = k0; m1_ = k1_; }
      red[(w4 * 2 + 0) * 64 + lane] = t0;
      red[(w4 * 2 + 1) * 64 + lane] = t1;
      __syncthreads();
      m0_ += red[((w4 ^ 1) * 2 + 0) * 64 + lane];
      m1_ += red[((w4 ^ 1) * 2 + 1) * 64 + lane];
      const float dsk = bf2f(Dsk[d0 + ch]);
      const int tb = 2 * w4;
      {
        int l = 16 * (16 * tb + r16) + 4 * q;
        s16x4 ov;
#pragma unroll
        for (int r = 0; r < 4; r++) ov[r] = (short)f2bf(rbf(m0_[r]) + rbf(bf2f(us[256 + l + r]) * dsk));
        if (COH) st_coh64(aout + (size_t)(d0 + ch) * L_SEQ + l, __builtin_bit_cast(u64, ov));
        else *reinterpret_cast<s16x4*>(aout + (size_t)(d0 + ch) * L_SEQ + l) = ov;
        l += 256;
#pragma unroll
        for (int r = 0; r < 4; r++) ov[r] = (short)f2bf(rbf(m1_[r]) + rbf(bf2f(us[256 + l + r]) * dsk));
        if (COH) st_coh64(aout + (size_t)(d0 + ch) * L_SEQ + l, __builtin_bit_cast(u64, ov));
        else *reinterpret_cast<s16x4*>(aout + (size_t)(d0 + ch) * L_SEQ + l) = ov;
      }
    }
    __syncthreads();
    STAMP1(dbase, 4 + pass);
  }
}

__global__ void __launch_bounds__(NTHR, 4) k1(const u16* __restrict__ x, const u16* __restrict__ logAre,
                                          const u16* __restrict__ Aim, const u16* __restrict__ Cre,
                                          const u16* __restrict__ Cim, const u16* __restrict__ logdt,
                                          const u16* __restrict__ Dsk, const u16* __restrict__ w1,
                                          const u16* __restrict__ gw, u16* __restrict__ h,
                                          u16* __restrict__ aout, float* __restrict__ g,
                                          const u16* __restrict__ w2, u16* __restrict__ w2p) {
  __shared__ __align__(16) char smem[sizeof(ConvLds) > sizeof(G1Lds) ? sizeof(ConvLds) : sizeof(G1Lds)];
  int bid = blockIdx.x;
  if (bid < CONV_BLOCKS) {
    conv_block<true>(bid, x, logAre, Aim, Cre, Cim, logdt, Dsk, aout, smem);
    return;
    conv_block<false>(bid, x, logAre, Aim, Cre, Cim, logdt, Dsk, aout, smem);
  } else if (bid < CONV_BLOCKS + G1_BLOCKS) {
    gemm1_block(bid - CONV_BLOCKS, x, w1, h, smem);
  } else {
    side_block(bid - CONV_BLOCKS - G1_BLOCKS, x, gw, g, w2, w2p);
  }
}

#define G2_BM 64
#define G2_BN 80
#define G2_BK 64
#define G2_TILES_N (D_CH / G2_BN)
#define G2_TILES_M (L_SEQ / G2_BM)
#define G2_NST 7
#define G2_NK (F_FF / G2_BK)
#define G2_OLD 88

#define G2_STAGE_BYTES (8192 + 10240)
struct __align__(16) G2Lds {
  union {
    u16 st[G2_NST][G2_STAGE_BYTES / 2];
    struct {
      float red[2][2][5][64][4];
      u16 ot[G2_BM][G2_OLD];
    } e;
  } un;
};

__global__ void __launch_bounds__(384) k2(const u16* __restrict__ h, const u16* __restrict__ w2,
                                          const u16* __restrict__ aout, const float* __restrict__ g,
                                          u16* __restrict__ out) {
  __shared__ G2Lds S;
  const int tid = threadIdx.x;
  const int lane = tid & 63;
  const int wave = tid >> 6;
  const int bid = blockIdx.x;
  const int xcd = bid & 7, loc = bid >> 3;
  const int tm = 4 * xcd + (loc >> 3), tn = loc & 7;
  const int m0 = tm * G2_BM, n0 = tn * G2_BN;
  const int rot = (3 * tn + 7 * (tm & 3)) % G2_NK;
  char* lbase = reinterpret_cast<char*>(&S.un.st[0][0]);
#define STAMP(i)

  if (wave >= 4) {
    const int ld = wave - 4;
    const u16* asrc;
    {
      int off = ld * 4096 + lane * 16;
      int r = off >> 7, pc = (off >> 4) & 7;
      int c = pc ^ (r & 7);
      asrc = h + (size_t)(m0 + r) * F_FF + c * 8;
    }
    const u16* bsrc = w2 + (size_t)tn * (G2_NK * 5120) + 5 * ld * 512 + lane * 8;
#define G2_ISSUE(it_, buf_)                                                                        \
  {                                                                                                \
    int kk = (it_) + rot; if (kk >= G2_NK) kk -= G2_NK;                                            \
    char* lb = lbase + (buf_) * G2_STAGE_BYTES;                                                    \
    _Pragma("unroll") for (int i = 0; i < 4; i++)                                                  \
      __builtin_amdgcn_global_load_lds((const unsigned*)(asrc + (size_t)i * 8 * F_FF + kk * G2_BK), \
                                       (unsigned*)(lb + (4 * ld + i) * 1024), 16, 0, 0);           \
    _Pragma("unroll") for (int i = 0; i < 5; i++)                                                  \
      __builtin_amdgcn_global_load_lds((const unsigned*)(bsrc + kk * 5120 + i * 512),              \
                                       (unsigned*)(lb + 8192 + (5 * ld + i) * 1024), 16, 0, 0);    \
  }
#pragma unroll
    for (int s = 0; s < G2_NST - 1; s++) G2_ISSUE(s, s);
    asm volatile("s_waitcnt vmcnt(45)" ::: "memory");
    __builtin_amdgcn_s_barrier();
    int bp = G2_NST - 1;
    for (int it = 0; it < G2_NK; it++) {
      asm volatile("s_waitcnt vmcnt(36)" ::: "memory");
      __builtin_amdgcn_s_barrier();
      int nx = it + G2_NST - 1;
      if (nx < G2_NK) { G2_ISSUE(nx, bp); }
      else asm volatile("s_waitcnt vmcnt(0)" ::: "memory");
      bp = bp + 1; if (bp >= G2_NST) bp = 0;
    }
    asm volatile("s_waitcnt vmcnt(0)" ::: "memory");
    __syncthreads();
    __syncthreads();
  } else {
    const int wm = wave & 1, wk = wave >> 1;
    f32x4 acc[2][5];
#pragma unroll
    for (int i = 0; i < 2; i++)
#pragma unroll
      for (int j = 0; j < 5; j++) acc[i][j] = (f32x4){0.f, 0.f, 0.f, 0.f};
    const int q = lane >> 4, ii = lane & 15;
    const int boff0 = 8192 + ((wk * 32 + 8 * q + (ii >> 2) + 4 * (q & 1)) * 80 + 4 * (ii & 3)) * 2;
    const int boff1 = 8192 + ((wk * 32 + 8 * q + (ii >> 2) + 4 * ((q & 1) ^ 1)) * 80 + 4 * (ii & 3)) * 2;
#define G2_FRAGS(AF, BF, buf_)                                                                     \
  {                                                                                                \
    const char* lb = lbase + (buf_) * G2_STAGE_BYTES;                                              \
    _Pragma("unroll") for (int i = 0; i < 2; i++) {                                                \
      int row = wm * 32 + i * 16 + (lane & 15);                                                    \
      int pc = (wk * 4 + (lane >> 4)) ^ (row & 7);                                                 \
      AF[i] = *reinterpret_cast<const bf16x8*>(lb + row * 128 + pc * 16);                          \
    }                                                                                              \
    _Pragma("unroll") for (int j = 0; j < 5; j++) {                                                \
      s16x4 x0 = lds_tr16(reinterpret_cast<const u16*>(lb + boff0 + j * 32));                      \
      s16x4 x1 = lds_tr16(reinterpret_cast<const u16*>(lb + boff1 + j * 32));                      \
      s16x4 lo = (q & 1) ? x1 : x0, hi = (q & 1) ? x0 : x1;                                        \
      BF[j] = (bf16x8){lo[0], lo[1], lo[2], lo[3], hi[0], hi[1], hi[2], hi[3]};                    \
    }                                                                                              \
  }
    s16x4 av4[5];
    f32x4 g4;
    const int row0 = m0 + wm * 32 + wk * 16 + 4 * q;
#pragma unroll
    for (int j = 0; j < 5; j++)
      av4[j] = *reinterpret_cast<const s16x4*>(aout + (size_t)(n0 + j * 16 + ii) * L_SEQ + row0);
    g4 = *reinterpret_cast<const f32x4*>(g + row0);

    bf16x8 af[2], bfr[5], an[2], bn[5];
    __builtin_amdgcn_s_barrier();
    STAMP(1);
    G2_FRAGS(af, bfr, 0);
    int b1 = 1;
    for (int it = 0; it < G2_NK; it++) {
      asm volatile("s_waitcnt lgkmcnt(0)" ::: "memory");
      __builtin_amdgcn_s_barrier();
      G2_FRAGS(an, bn, b1);
#pragma unroll
      for (int i = 0; i < 2; i++)
#pragma unroll
        for (int j = 0; j < 5; j++)
          acc[i][j] = __builtin_amdgcn_mfma_f32_16x16x32_bf16(af[i], bfr[j], acc[i][j], 0, 0, 0);
#pragma unroll
      for (int i = 0; i < 2; i++) af[i] = an[i];
#pragma unroll
      for (int j = 0; j < 5; j++) bfr[j] = bn[j];
      b1 = b1 + 1; if (b1 >= G2_NST) b1 = 0;
    }
    STAMP(2);
    __syncthreads();
#pragma unroll
    for (int j = 0; j < 5; j++)
      *reinterpret_cast<f32x4*>(&S.un.e.red[wm][wk][j][lane][0]) = wk ? acc[0][j] : acc[1][j];
    __syncthreads();
#pragma unroll
    for (int j = 0; j < 5; j++) {
      f32x4 o = *reinterpret_cast<const f32x4*>(&S.un.e.red[wm][wk ^ 1][j][lane][0]);
      f32x4 mine = wk ? acc[1][j] : acc[0][j];
#pragma unroll
      for (int r = 0; r < 4; r++) {
        float b = rbf(mine[r] + o[r]);
        float av = bf2f((u16)av4[j][r]);
        float t1 = rbf(g4[r] * av);
        float t2 = rbf(rbf(1.f - g4[r]) * b);
        S.un.e.ot[wm * 32 + wk * 16 + 4 * q + r][j * 16 + ii] = f2bf(t1 + t2);
      }
    }
  }
  __syncthreads();
  for (int c = tid; c < 640; c += 384) {
    int r = c / 10, cc = c - r * 10;
    u32x4 v = *reinterpret_cast<const u32x4*>(&S.un.e.ot[r][cc * 8]);
    *reinterpret_cast<u32x4*>(out + (size_t)(m0 + r) * D_CH + n0 + cc * 8) = v;
  }
  STAMP(3);
}

extern "C" void kernel_launch(void* const* d_in, const int* in_sizes, int n_in,
                              void* d_out, int out_size, void* d_ws, size_t ws_size,
                              hipStream_t stream) {
  const u16* u = (const u16*)d_in[0];
  const u16* logAre = (const u16*)d_in[1];
  const u16* Aim = (const u16*)d_in[2];
  const u16* Cre = (const u16*)d_in[3];
  const u16* Cim = (const u16*)d_in[4];
  const u16* logdt = (const u16*)d_in[5];
  const u16* Dsk = (const u16*)d_in[6];
  const u16* w1 = (const u16*)d_in[8];
  const u16* w2 = (const u16*)d_in[9];
  const u16* gw = (const u16*)d_in[10];
  u16* out = (u16*)d_out;
  char* ws = (char*)d_ws;
  u16* h = (u16*)ws;
  u16* aout = (u16*)(ws + (8u << 20));
  float* g = (float*)(ws + (12u << 20));
  u16* w2p = (u16*)(ws + (16u << 20));

  k1<<<CONV_BLOCKS + G1_BLOCKS + SIDE_BLOCKS, NTHR, 0, stream>>>(u, logAre, Aim, Cre, Cim, logdt, Dsk,
                                                                 w1, gw, h, aout, g, w2, w2p);
  k2<<<G2_TILES_M * G2_TILES_N, 384, 0, stream>>>(h, w2p, aout, g, out);
}
```

```cpp
#include <hip/hip_runtime.h>
#include <hip/hip_bf16.h>
#include <stdint.h>

#define L_SEQ 2048
#define D_CH 640
#define N_ST 128
#define F_FF 1728

typedef unsigned short u16;
#define STAMP1(base, i)
typedef __attribute__((ext_vector_type(8))) short bf16x8;
typedef __attribute__((ext_vector_type(4))) short s16x4;
typedef __attribute__((ext_vector_type(4))) float f32x4;
typedef __attribute__((ext_vector_type(4))) unsigned u32x4;

__device__ __forceinline__ float bf2f(u16 v) { return __uint_as_float(((unsigned)v) << 16); }
typedef __attribute__((ext_vector_type(2))) __bf16 bf16v2;
typedef __attribute__((ext_vector_type(2))) float f32x2;
__device__ __forceinline__ unsigned pk_bf(float a, float b) {
  f32x2 v = {a, b};
  return __builtin_bit_cast(unsigned, __builtin_convertvector(v, bf16v2));
}
__device__ __forceinline__ u16 f2bf(float f) { return (u16)(pk_bf(f, f) & 0xffffu); }
__device__ __forceinline__ float rbf(float f) { return __uint_as_float(pk_bf(f, f) << 16); }

__device__ __forceinline__ s16x4 lds_tr16(const u16* p) {
  return __builtin_amdgcn_ds_read_tr16_b64_v4i16((__attribute__((address_space(3))) s16x4*)(p));
}

#define NTHR 512
#define G1_BM 128
#define G1_BN 192
#define G1_BK 64
#define G1_TILES_N (F_FF / G1_BN)
#define G1_BLOCKS 144
#define G1_NST 2
#define G1_NK (D_CH / G1_BK)
#define G1_OLD 200
#define CONV_BLOCKS (D_CH / 2)
#define NKS (F_FF / 32)

typedef unsigned long long u64;
__device__ __forceinline__ void st_coh64(void* p, u64 v) {
  __hip_atomic_store(reinterpret_cast<u64*>(p), v, __ATOMIC_RELAXED, __HIP_MEMORY_SCOPE_AGENT);
}
__device__ __forceinline__ void st_wt128(void* p, u32x4 v) {
  asm volatile("global_store_dwordx4 %0, %1, off sc1\n\ts_nop 1" :: "v"(p), "v"(v) : "memory");
}
__device__ __forceinline__ u64 ld_coh64(const void* p) {
  return __hip_atomic_load(reinterpret_cast<const u64*>(p), __ATOMIC_RELAXED, __HIP_MEMORY_SCOPE_AGENT);
}
__device__ __forceinline__ u16 ld_coh16(const u16* p) {
  return __hip_atomic_load(p, __ATOMIC_RELAXED, __HIP_MEMORY_SCOPE_AGENT);
}
__device__ __forceinline__ float lo_f(unsigned w) { return __uint_as_float(w << 16); }
__device__ __forceinline__ float hi_f(unsigned w) { return __uint_as_float(w & 0xffff0000u); }

#define CV_US_ELEMS (256 + L_SEQ)
#define CV_R_ELEMS 2080

struct __align__(16) ConvLds {
  u16 us[2][CV_US_ELEMS];
  f32x4 prmA[2][N_ST];
  float prmB[2][N_ST];
  float ksum[2][768];
  union {
    float kp[2][4][768];
    u16 r[2][4][CV_R_ELEMS];
  } un;
};

#define G1_STAGE_BYTES (16384 + 24576)
struct __align__(16) G1Lds {
  union {
    u16 st[G1_NST][G1_STAGE_BYTES / 2];
    u16 ot[G1_BM][G1_OLD];
  } un;
};

__device__ __forceinline__ float silu_f(float v) {
  return v * __builtin_amdgcn_rcpf(1.f + __builtin_amdgcn_exp2f(v * -1.4426950408889634f));
}
__device__ __forceinline__ unsigned silu_pk(float a, float b) {
  unsigned w = pk_bf(a, b);
  return pk_bf(silu_f(lo_f(w)), silu_f(hi_f(w)));
}

__device__ __forceinline__ void gemm1_block(int bid, const u16* __restrict__ x, const u16* __restrict__ w1,
                            u16* __restrict__ h, char* smem) {
  G1Lds& S = *reinterpret_cast<G1Lds*>(smem);
  const int tid = threadIdx.x;
  const int lane = tid & 63;
  const int wave = tid >> 6;
  const int xcd = bid & 7, loc = bid >> 3;
  const int tm = 2 * xcd + loc / G1_TILES_N, tn = loc % G1_TILES_N;
  const int m0 = tm * G1_BM, n0 = tn * G1_BN;
  const int rot = (tn + 5 * (tm & 1)) % G1_NK;
  char* lbase = reinterpret_cast<char*>(&S.un.st[0][0]);
  const int dslot = 320 + bid; (void)dslot;
  STAMP1(h, 0);
  __builtin_amdgcn_s_setprio(1);

  unsigned aofs, bofs[3];
  {
    int off = wave * 2048 + lane * 16;
    int r = off >> 7, pc = (off >> 4) & 7;
    aofs = (unsigned)((m0 + r) * D_CH + ((pc ^ (r & 7)) * 8));
#pragma unroll
    for (int i = 0; i < 3; i++) {
      int o2 = (3 * wave + i) * 1024 + lane * 16;
      int rb = o2 / 384, cp = (o2 % 384) >> 4;
      int c = cp ^ (2 * ((rb >> 1) & 1));
      bofs[i] = (unsigned)(rb * F_FF + n0 + c * 8);
    }
  }
#define G1_ISSUE(it_, buf_)                                                                        \
  {                                                                                                \
    int kk = (it_) + rot; if (kk >= G1_NK) kk -= G1_NK;                                            \
    char* lb = lbase + (buf_) * G1_STAGE_BYTES;                                                    \
    const u16* xa = x + kk * G1_BK;                                                                \
    const u16* wb = w1 + (size_t)kk * (G1_BK * F_FF);                                              \
    __builtin_amdgcn_global_load_lds((const unsigned*)(xa + aofs),                                 \
                                     (unsigned*)(lb + wave * 2048), 16, 0, 0);                     \
    __builtin_amdgcn_global_load_lds((const unsigned*)(xa + aofs + 8 * D_CH),                      \
                                     (unsigned*)(lb + wave * 2048 + 1024), 16, 0, 0);              \
    _Pragma("unroll") for (int i = 0; i < 3; i++)                                                  \
      __builtin_amdgcn_global_load_lds((const unsigned*)(wb + bofs[i]),                            \
                                       (unsigned*)(lb + 16384 + (3 * wave + i) * 1024), 16, 0, 0); \
  }
  G1_ISSUE(0, 0);

  const int wm = wave & 3, wn = wave >> 2;
  const int q = lane >> 4, ii = lane & 15;
  f32x4 acc[2][6];
#pragma unroll
  for (int i = 0; i < 2; i++)
#pragma unroll
    for (int j = 0; j < 6; j++) acc[i][j] = (f32x4){0.f, 0.f, 0.f, 0.f};
  const int aoff = (wm * 32 + ii) * 128 + ((q ^ (ii & 7)) * 16);
  int boff[6];
#pragma unroll
  for (int j = 0; j < 6; j++)
    boff[j] = 16384 + (8 * q + (ii >> 2)) * 384 + (((wn * 6 + j) ^ ((ii >> 3) & 1)) * 32) + 8 * (ii & 3);

  int bc = 0, bp = 1;
  for (int it = 0; it < G1_NK; it++) {
    asm volatile("s_waitcnt vmcnt(0) lgkmcnt(0)" ::: "memory");
    __builtin_amdgcn_s_barrier();
    asm volatile("" ::: "memory");
    if (it == 0) { STAMP1(h, 1); }
    if (it + 1 < G1_NK) { G1_ISSUE(it + 1, bp); }
    const char* lb = lbase + bc * G1_STAGE_BYTES;
#pragma unroll
    for (int hk = 0; hk < 2; hk++) {
      bf16x8 af[2], bfr[6];
#pragma unroll
      for (int i = 0; i < 2; i++)
        af[i] = *reinterpret_cast<const bf16x8*>(lb + (aoff ^ (hk * 64)) + i * 2048);
#pragma unroll
      for (int j = 0; j < 6; j++) {
        const char* p0 = lb + boff[j] + hk * (32 * 384);
        s16x4 lo = lds_tr16(reinterpret_cast<const u16*>(p0));
        s16x4 hi = lds_tr16(reinterpret_cast<const u16*>(p0 + 4 * 384));
        bfr[j] = (bf16x8){lo[0], lo[1], lo[2], lo[3], hi[0], hi[1], hi[2], hi[3]};
      }
#pragma unroll
      for (int i = 0; i < 2; i++)
#pragma unroll
        for (int j = 0; j < 6; j++)
          acc[i][j] = __builtin_amdgcn_mfma_f32_16x16x32_bf16(bfr[j], af[i], acc[i][j], 0, 0, 0);
    }
    bc = bc + 1; if (bc >= G1_NST) bc = 0;
    bp = bp + 1; if (bp >= G1_NST) bp = 0;
  }
  STAMP1(h, 2);
#pragma unroll
  for (int i = 0; i < 2; i++) {
    const int rb = (m0 >> 4) + wm * 2 + i;
#pragma unroll
    for (int jp = 0; jp < 3; jp++) {
      u32x4 v;
      v[0] = silu_pk(acc[i][2 * jp][0], acc[i][2 * jp][1]);
      v[1] = silu_pk(acc[i][2 * jp][2], acc[i][2 * jp][3]);
      v[2] = silu_pk(acc[i][2 * jp + 1][0], acc[i][2 * jp + 1][1]);
      v[3] = silu_pk(acc[i][2 * jp + 1][2], acc[i][2 * jp + 1][3]);
      const int ks = (n0 >> 5) + wn * 3 + jp;
      st_wt128(h + (size_t)(((((rb >> 2) * NKS + ks) * 4 + (rb & 3)) * 64 + lane) * 8), v);
    }
  }
  STAMP1(h, 3);
}

#define SIDE_BLOCKS 48
__device__ __forceinline__ void side_block(int bid, const u16* __restrict__ x, const u16* __restrict__ gw,
                                           float* __restrict__ g, const u16* __restrict__ w2,
                                           u16* __restrict__ w2p) {
  const int tid = threadIdx.x;
  const int lane = tid & 63;
  const int wave = tid >> 6;
  const int q = lane >> 4, ii = lane & 15;
  const unsigned* gw32 = reinterpret_cast<const unsigned*>(gw);
  unsigned gwv[5], xv[6][5];
#pragma unroll
  for (int i = 0; i < 5; i++) gwv[i] = gw32[lane + 64 * i];
#pragma unroll
  for (int rr = 0; rr < 6; rr++) {
    int row = bid * 43 + wave * 6 + rr;
    if (row > L_SEQ - 1) row = L_SEQ - 1;
    const unsigned* xr = reinterpret_cast<const unsigned*>(x + (size_t)row * D_CH);
#pragma unroll
    for (int i = 0; i < 5; i++) xv[rr][i] = xr[lane + 64 * i];
  }
  for (int un = bid * 8 + wave; un < NKS * 8; un += SIDE_BLOCKS * 8) {
    const int ks = un >> 3, tn = un & 7;
    const u16* src = w2 + (size_t)(ks * 32 + 4 * q) * D_CH + tn * 80 + ii;
    u16 e[5][8];
#pragma unroll
    for (int j = 0; j < 5; j++)
#pragma unroll
      for (int c = 0; c < 8; c++) e[j][c] = src[(c < 4 ? c : 12 + c) * D_CH + j * 16];
#pragma unroll
    for (int j = 0; j < 5; j++) {
      u32x4 v;
#pragma unroll
      for (int c = 0; c < 4; c++) v[c] = (unsigned)e[j][2 * c] | ((unsigned)e[j][2 * c + 1] << 16);
      st_wt128(w2p + (size_t)((((tn * NKS + ks) * 5 + j) * 64 + lane) * 8), v);
    }
  }
#pragma unroll
  for (int rr = 0; rr < 6; rr++) {
    int row = bid * 43 + wave * 6 + rr;
    float s = 0.f;
#pragma unroll
    for (int i = 0; i < 5; i++) {
      s += rbf(lo_f(xv[rr][i]) * lo_f(gwv[i]));
      s += rbf(hi_f(xv[rr][i]) * hi_f(gwv[i]));
    }
#pragma unroll
    for (int off = 32; off > 0; off >>= 1) s += __shfl_xor(s, off);
    if (lane == 0 && row < L_SEQ && wave * 6 + rr < 43) {
      float sv = rbf(s);
      g[row] = rbf(1.f / (1.f + __expf(-sv)));
    }
  }
}

template <bool COH>
__device__ __forceinline__ void conv_block(int blk, const u16* __restrict__ u, const u16* __restrict__ logAre,
                           const u16* __restrict__ Aim, const u16* __restrict__ Cre,
                           const u16* __restrict__ Cim, const u16* __restrict__ logdt,
                           const u16* __restrict__ Dsk, u16* __restrict__ aout, char* smem) {
  ConvLds& S = *reinterpret_cast<ConvLds*>(smem);
  const int tid = threadIdx.x;
  const int lane = tid & 63;
  const int wave = tid >> 6;
  const int cg = wave >> 2, w4 = wave & 3, gt = tid & 255;
  const int d0 = 2 * ((blk & 7) * 40 + (blk >> 3));
  char* dbase = (char*)aout - (8u << 20);
  const int dslot = blk; (void)dslot; (void)dbase;
  STAMP1(dbase, 0);
  __builtin_amdgcn_s_setprio(2);

  u16 p_ld = 0, p_la = 0, p_ai = 0, p_cr = 0, p_ci = 0;
  const int pch = (tid >> 7) & 1, pn = tid & 127;
  if (tid < 256) {
    const int d = d0 + pch;
    p_ld = logdt[d];
    p_la = logAre[d * N_ST + pn];
    p_ai = Aim[d * N_ST + pn];
    p_cr = Cre[d * N_ST + pn];
    p_ci = Cim[d * N_ST + pn];
  }
  unsigned ureg[4];
  if (tid < 256) {
    const float dt = rbf(expf(bf2f(p_ld)));
    float are = -rbf(expf(bf2f(p_la)));
    float aim = bf2f(p_ai);
    float cre = bf2f(p_cr) * dt;
    float cim = bf2f(p_ci) * dt;
    double al = (double)(are * dt) * 1.4426950408889634;
    double be = (double)(aim * dt) * 0.15915494309189535;
    float beh = __uint_as_float(__float_as_uint((float)be) & 0xfffff000u);
    float bel = (float)(be - (double)beh);
    float mx = fmaxf(fabsf(cre), fabsf(cim)), mn = fminf(fabsf(cre), fabsf(cim));
    float rt = mx > 0.f ? mn / mx : 0.f;
    float lnc = log2f(mx) + 0.5f * log2f(1.f + rt * rt) + 1.0f;
    float ph = atan2f(cim, cre) * 0.15915494309189535f;
    S.prmA[pch][pn] = (f32x4){(float)al, beh, bel, lnc};
    S.prmB[pch][pn] = ph;
  }
  __syncthreads();
  {
    int uo = tid * D_CH + d0;
    asm volatile("" : "+v"(uo) :: "memory");
#pragma unroll
    for (int i = 0; i < 4; i++)
      ureg[i] = *reinterpret_cast<const unsigned*>(u + uo + NTHR * i * D_CH);
  }

  STAMP1(dbase, 1);
  const int r16 = lane & 15, q = lane >> 4;
  {
    const int pass = 0;
    const int ch = cg;
    {
      float pr[3];
      pr[0] = (float)r16;
      pr[1] = (r16 < 8) ? (float)(256 + 2 * r16) : (float)(512 + 4 * (r16 & 3) + 256 * ((r16 - 8) >> 2));
      pr[2] = (r16 < 8) ? (float)(1024 + 8 * (r16 & 1) + 256 * (r16 >> 1)) : 2048.f;
      const float pc = (float)(16 * r16);
      f32x4 acc[3];
      u32x4 kah[3], kal[3], kbh, kbl;
#pragma unroll
      for (int i = 0; i < 3; i++) acc[i] = (f32x4){0.f, 0.f, 0.f, 0.f};
#pragma unroll
      for (int s = 0; s < 8; s++) {
        int n = 32 * w4 + 4 * s + q;
        f32x4 pv = S.prmA[ch][n];
        float ph = S.prmB[ch][n];
        float al = pv[0], beh = pv[1], bel = pv[2], lnc = pv[3];
        f32x2 z0, z1, z2, zc;
        {
          float t = __builtin_amdgcn_fractf(beh * pc);
          t = fmaf(bel, pc, t);
          float mg = __builtin_amdgcn_exp2f(al * pc);
          zc[0] = mg * __builtin_amdgcn_cosf(t);
          zc[1] = -mg * __builtin_amdgcn_sinf(t);
        }
#define KG1(Z, I) { float t = __builtin_amdgcn_fractf(beh * pr[I]); t = fmaf(bel, pr[I], t + ph); \
          float mg = __builtin_amdgcn_exp2f(fmaf(al, pr[I], lnc)); \
          Z[0] = mg * __builtin_amdgcn_cosf(t); Z[1] = mg * __builtin_amdgcn_sinf(t); }
        KG1(z0, 0) KG1(z1, 1) KG1(z2, 2)
        {
          const int j = s & 3;
          unsigned h_;
          h_ = pk_bf(z0[0], z0[1]); kah[0][j] = h_; kal[0][j] = pk_bf(z0[0] - lo_f(h_), z0[1] - hi_f(h_));
          h_ = pk_bf(z1[0], z1[1]); kah[1][j] = h_; kal[1][j] = pk_bf(z1[0] - lo_f(h_), z1[1] - hi_f(h_));
          h_ = pk_bf(z2[0], z2[1]); kah[2][j] = h_; kal[2][j] = pk_bf(z2[0] - lo_f(h_), z2[1] - hi_f(h_));
          h_ = pk_bf(zc[0], zc[1]); kbh[j] = h_; kbl[j] = pk_bf(zc[0] - lo_f(h_), zc[1] - hi_f(h_));
          if (j == 3) {
#pragma unroll
            for (int i = 0; i < 3; i++)
              acc[i] = __builtin_amdgcn_mfma_f32_16x16x32_bf16(__builtin_bit_cast(bf16x8, kal[i]), __builtin_bit_cast(bf16x8, kbh), acc[i], 0, 0, 0);
#pragma unroll
            for (int i = 0; i < 3; i++)
              acc[i] = __builtin_amdgcn_mfma_f32_16x16x32_bf16(__builtin_bit_cast(bf16x8, kah[i]), __builtin_bit_cast(bf16x8, kbl), acc[i], 0, 0, 0);
#pragma unroll
            for (int i = 0; i < 3; i++)
              acc[i] = __builtin_amdgcn_mfma_f32_16x16x32_bf16(__builtin_bit_cast(bf16x8, kah[i]), __builtin_bit_cast(bf16x8, kbh), acc[i], 0, 0, 0);
          }
        }
      }
#pragma unroll
      for (int i = 0; i < 3; i++)
#pragma unroll
        for (int j = 0; j < 4; j++) S.un.kp[cg][w4][(16 * i + 4 * q + j) * 16 + r16] = acc[i][j];
    }
    if (pass == 0) {
      STAMP1(dbase, 6);
      S.us[tid >> 8][tid & 255] = 0;
#pragma unroll
      for (int i = 0; i < 4; i++) {
        int l = 256 + tid + NTHR * i;
        S.us[0][l] = (u16)(ureg[i] & 0xffff);
        S.us[1][l] = (u16)(ureg[i] >> 16);
      }
    }
    __syncthreads();
    if (pass == 0) STAMP1(dbase, 2);
    {
      const int c = gt & 15, rr = gt >> 4;
      int idx[3];
      idx[0] = rr + 16 * c;
      idx[1] = (rr < 8) ? 256 + rr + 8 * c : 384 + (rr & 3) + 64 * ((rr - 8) >> 2) + 4 * c;
      idx[2] = (rr < 8) ? 512 + (rr & 1) + 32 * (rr >> 1) + 2 * c : (gt == 128 ? 640 : 641);
#pragma unroll
      for (int i = 0; i < 3; i++) {
        int e = gt + 256 * i;
        S.ksum[cg][idx[i]] = (S.un.kp[cg][0][e] + S.un.kp[cg][1][e]) + (S.un.kp[cg][2][e] + S.un.kp[cg][3][e]);
      }
    }
    __syncthreads();
    {
      const int sh = gt < 128 ? 3 : (gt < 192 ? 2 : (gt < 224 ? 1 : 0));
      const int add = sh ? (1 << (sh - 1)) - 1 : 0;
      const int lsb = sh ? 1 : 0;
      const float* tb = &S.ksum[cg][128 * sh];
      float kv[9];
#pragma unroll
      for (int e = 0; e < 9; e++) {
        int m = 2047 - 8 * gt - e;
        int mm = m < 0 ? 0 : m;
        kv[e] = tb[(mm + add + ((mm >> sh) & lsb)) >> sh];
      }
      kv[8] = (gt == 255) ? 0.f : kv[8];
      u32x4 h0, h1, l0, l1;
      float lo[9];
#pragma unroll
      for (int e = 0; e < 4; e++) {
        h0[e] = pk_bf(kv[2 * e], kv[2 * e + 1]);
        h1[e] = pk_bf(kv[2 * e + 1], kv[2 * e + 2]);
        lo[2 * e] = kv[2 * e] - lo_f(h0[e]);
        lo[2 * e + 1] = kv[2 * e + 1] - hi_f(h0[e]);
      }
      lo[8] = kv[8] - hi_f(h1[3]);
#pragma unroll
      for (int e = 0; e < 4; e++) {
        l0[e] = pk_bf(lo[2 * e], lo[2 * e + 1]);
        l1[e] = pk_bf(lo[2 * e + 1], lo[2 * e + 2]);
      }
      *reinterpret_cast<u32x4*>(&S.un.r[cg][0][8 * gt]) = h0;
      *reinterpret_cast<u32x4*>(&S.un.r[cg][1][8 * gt]) = h1;
      *reinterpret_cast<u32x4*>(&S.un.r[cg][2][8 * gt]) = l0;
      *reinterpret_cast<u32x4*>(&S.un.r[cg][3][8 * gt]) = l1;
      if (gt < 16) *reinterpret_cast<u32x4*>(&S.un.r[cg][gt >> 2][2048 + 8 * (gt & 3)]) = (u32x4){0u, 0u, 0u, 0u};
    }
    __syncthreads();

    if (pass == 0) STAMP1(dbase, 3);
    __builtin_amdgcn_s_setprio(0);
    const u16* us = S.us[ch];
    f32x4 acc[8];
#pragma unroll
    for (int t = 0; t < 8; t++) acc[t] = (f32x4){0.f, 0.f, 0.f, 0.f};
    for (int kk = 0; kk < 2; kk++) {
      const int kp_ = 4 * w4 + 2 * kk;
      bf16x8 bfr[8];
#pragma unroll
      for (int dl = 0; dl < 8; dl++)
        bfr[dl] = *reinterpret_cast<const bf16x8*>(&us[256 + 16 * (16 * dl - kp_ + r16 - 1) + 8 * q]);
      const int xx = 2047 - (16 * (kp_ + 1) + r16 - 8 * q);
#pragma unroll
      for (int part = 0; part < 2; part++) {
        const unsigned* rp = (xx & 1) ? reinterpret_cast<const unsigned*>(&S.un.r[cg][2 * part + 1][xx - 1])
                                      : reinterpret_cast<const unsigned*>(&S.un.r[cg][2 * part][xx]);
        u32x4 af[8];
#pragma unroll
        for (int kb = 0; kb < 8; kb++)
          af[kb] = (u32x4){rp[0 - 128 * kb], rp[1 - 128 * kb], rp[2 - 128 * kb], rp[3 - 128 * kb]};
#pragma unroll
        for (int kb = 0; kb < 8; kb++)
#pragma unroll
          for (int t = kb; t < 8; t++)
            acc[t] = __builtin_amdgcn_mfma_f32_16x16x32_bf16(__builtin_bit_cast(bf16x8, af[kb]), bfr[t - kb], acc[t], 0, 0, 0);
      }
    }
    __syncthreads();
    f32x4* red = reinterpret_cast<f32x4*>(&S.un.r[cg][0][0]);
    {
      const bool hi_half = (w4 >> 1) != 0;
      f32x4 s0, s1, s2, s3, k0, k1_, k2_, k3;
      if (hi_half) { s0 = acc[0]; s1 = acc[1]; s2 = acc[2]; s3 = acc[3]; k0 = acc[4]; k1_ = acc[5]; k2_ = acc[6]; k3 = acc[7]; }
      else { s0 = acc[4]; s1 = acc[5]; s2 = acc[6]; s3 = acc[7]; k0 = acc[0]; k1_ = acc[1]; k2_ = acc[2]; k3 = acc[3]; }
      red[(w4 * 4 + 0) * 64 + lane] = s0;
      red[(w4 * 4 + 1) * 64 + lane] = s1;
      red[(w4 * 4 + 2) * 64 + lane] = s2;
      red[(w4 * 4 + 3) * 64 + lane] = s3;
      __syncthreads();
      k0 += red[((w4 ^ 2) * 4 + 0) * 64 + lane];
      k1_ += red[((w4 ^ 2) * 4 + 1) * 64 + lane];
      k2_ += red[((w4 ^ 2) * 4 + 2) * 64 + lane];
      k3 += red[((w4 ^ 2) * 4 + 3) * 64 + lane];
      __syncthreads();
      const bool od = (w4 & 1) != 0;
      f32x4 t0, t1, m0_, m1_;
      if (od) { t0 = k0; t1 = k1_; m0_ = k2_; m1_ = k3; }
      else { t0 = k2_; t1 = k3; m0_ = k0; m1_ = k1_; }
      red[(w4 * 2 + 0) * 64 + lane] = t0;
      red[(w4 * 2 + 1) * 64 + lane] = t1;
      __syncthreads();
      m0_ += red[((w4 ^ 1) * 2 + 0) * 64 + lane];
      m1_ += red[((w4 ^ 1) * 2 + 1) * 64 + lane];
      const float dsk = bf2f(Dsk[d0 + ch]);
      const int tb = 2 * w4;
      {
        int l = 16 * (16 * tb + r16) + 4 * q;
        s16x4 ov;
#pragma unroll
        for (int r = 0; r < 4; r++) ov[r] = (short)f2bf(rbf(m0_[r]) + rbf(bf2f(us[256 + l + r]) * dsk));
        if (COH) st_coh64(aout + (size_t)(d0 + ch) * L_SEQ + l, __builtin_bit_cast(u64, ov));
        else *reinterpret_cast<s16x4*>(aout + (size_t)(d0 + ch) * L_SEQ + l) = ov;
        l += 256;
#pragma unroll
        for (int r = 0; r < 4; r++) ov[r] = (short)f2bf(rbf(m1_[r]) + rbf(bf2f(us[256 + l + r]) * dsk));
        if (COH) st_coh64(aout + (size_t)(d0 + ch) * L_SEQ + l, __builtin_bit_cast(u64, ov));
        else *reinterpret_cast<s16x4*>(aout + (size_t)(d0 + ch) * L_SEQ + l) = ov;
      }
    }
    __syncthreads();
    STAMP1(dbase, 4 + pass);
  }
}

__global__ void __launch_bounds__(NTHR, 4) k1(const u16* __restrict__ x, const u16* __restrict__ logAre,
                                          const u16* __restrict__ Aim, const u16* __restrict__ Cre,
                                          const u16* __restrict__ Cim, const u16* __restrict__ logdt,
                                          const u16* __restrict__ Dsk, const u16* __restrict__ w1,
                                          const u16* __restrict__ gw, u16* __restrict__ h,
                                          u16* __restrict__ aout, float* __restrict__ g,
                                          const u16* __restrict__ w2, u16* __restrict__ w2p) {
  __shared__ __align__(16) char smem[sizeof(ConvLds) > sizeof(G1Lds) ? sizeof(ConvLds) : sizeof(G1Lds)];
  int bid = blockIdx.x;
  if (bid < CONV_BLOCKS) {
    conv_block<true>(bid, x, logAre, Aim, Cre, Cim, logdt, Dsk, aout, smem);
    return;
    conv_block<false>(bid, x, logAre, Aim, Cre, Cim, logdt, Dsk, aout, smem);
  } else if (bid < CONV_BLOCKS + G1_BLOCKS) {
    gemm1_block(bid - CONV_BLOCKS, x, w1, h, smem);
  } else {
    side_block(bid - CONV_BLOCKS - G1_BLOCKS, x, gw, g, w2, w2p);
  }
}

#define G2_BM 64
#define G2_BN 80
#define G2_TILES_N (D_CH / G2_BN)
#define G2_TILES_M (L_SEQ / G2_BM)
#define G2_OLD 88
#define G2_PF 3

struct __align__(16) G2Lds {
  f32x4 red[4][3][5][64];
  u16 ot[G2_BM][G2_OLD];
};

__global__ void __launch_bounds__(256) k2(const u16* __restrict__ h, const u16* __restrict__ w2,
                                          const u16* __restrict__ aout, const float* __restrict__ g,
                                          u16* __restrict__ out) {
  __shared__ G2Lds S;
  const int tid = threadIdx.x;
  const int lane = tid & 63;
  const int wave = __builtin_amdgcn_readfirstlane(tid >> 6);
  const int bid = blockIdx.x;
  const int xcd = bid & 7, loc = bid >> 3;
  const int tm = 4 * xcd + (loc >> 3), tn = loc & 7;
  const int m0 = tm * G2_BM, n0 = tn * G2_BN;
#define STAMP(i)
  const int q = lane >> 4, ii = lane & 15;
  const int ks0 = wave < 2 ? 14 * wave : 28 + 13 * (wave - 2);
  const bool full = wave < 2;
  const u16* ap = h + (size_t)((tm * NKS + ks0) * 4 * 64 + lane) * 8;
  const u16* bp = w2 + (size_t)((tn * NKS + ks0) * 5 * 64 + lane) * 8;

  bf16x8 af[14][4], bfr[14][5];
#define G2_LOAD(s_)                                                                                \
  {                                                                                                \
    _Pragma("unroll") for (int i = 0; i < 4; i++)                                                  \
      af[s_][i] = *reinterpret_cast<const bf16x8*>(ap + ((s_) * 4 + i) * 512);                     \
    _Pragma("unroll") for (int j = 0; j < 5; j++)                                                  \
      bfr[s_][j] = *reinterpret_cast<const bf16x8*>(bp + ((s_) * 5 + j) * 512);                    \
  }
#pragma unroll
  for (int s = 0; s < G2_PF; s++) G2_LOAD(s);
  s16x4 av4[5];
  f32x4 g4;
  const int row0 = m0 + wave * 16 + 4 * q;
#pragma unroll
  for (int j = 0; j < 5; j++)
    av4[j] = *reinterpret_cast<const s16x4*>(aout + (size_t)(n0 + j * 16 + ii) * L_SEQ + row0);
  g4 = *reinterpret_cast<const f32x4*>(g + row0);
  __builtin_amdgcn_sched_barrier(0);

  f32x4 acc[4][5];
#pragma unroll
  for (int i = 0; i < 4; i++)
#pragma unroll
    for (int j = 0; j < 5; j++) acc[i][j] = (f32x4){0.f, 0.f, 0.f, 0.f};
#pragma unroll
  for (int s = 0; s < 13; s++) {
    if (s + G2_PF < 13) { G2_LOAD(s + G2_PF); }
    else if (s + G2_PF == 13) { if (full) { G2_LOAD(13); } }
    __builtin_amdgcn_sched_barrier(0);
#pragma unroll
    for (int i = 0; i < 4; i++)
#pragma unroll
      for (int j = 0; j < 5; j++)
        acc[i][j] = __builtin_amdgcn_mfma_f32_16x16x32_bf16(af[s][i], bfr[s][j], acc[i][j], 0, 0, 0);
    __builtin_amdgcn_sched_barrier(0);
    if (s == 0) { STAMP(1); }
  }
  if (full) {
#pragma unroll
    for (int i = 0; i < 4; i++)
#pragma unroll
      for (int j = 0; j < 5; j++)
        acc[i][j] = __builtin_amdgcn_mfma_f32_16x16x32_bf16(af[13][i], bfr[13][j], acc[i][j], 0, 0, 0);
  }
  STAMP(2);
  f32x4 mine[5];
#pragma unroll
  for (int i = 0; i < 4; i++) {
    if (i != wave) {
#pragma unroll
      for (int j = 0; j < 5; j++) S.red[i][(wave ^ i) - 1][j][lane] = acc[i][j];
    } else {
#pragma unroll
      for (int j = 0; j < 5; j++) mine[j] = acc[i][j];
    }
  }
  __syncthreads();
#pragma unroll
  for (int j = 0; j < 5; j++) {
    f32x4 t = (mine[j] + S.red[wave][0][j][lane]) + (S.red[wave][1][j][lane] + S.red[wave][2][j][lane]);
#pragma unroll
    for (int r = 0; r < 4; r++) {
      float b = rbf(t[r]);
      float av = bf2f((u16)av4[j][r]);
      float t1 = rbf(g4[r] * av);
      float t2 = rbf(rbf(1.f - g4[r]) * b);
      S.ot[wave * 16 + 4 * q + r][j * 16 + ii] = f2bf(t1 + t2);
    }
  }
  __syncthreads();
  for (int c = tid; c < 640; c += 256) {
    int r = c / 10, cc = c - r * 10;
    u32x4 v = *reinterpret_cast<const u32x4*>(&S.ot[r][cc * 8]);
    *reinterpret_cast<u32x4*>(out + (size_t)(m0 + r) * D_CH + n0 + cc * 8) = v;
  }
  STAMP(3);
}

extern "C" void kernel_launch(void* const* d_in, const int* in_sizes, int n_in,
                              void* d_out, int out_size, void* d_ws, size_t ws_size,
                              hipStream_t stream) {
  const u16* u = (const u16*)d_in[0];
  const u16* logAre = (const u16*)d_in[1];
  const u16* Aim = (const u16*)d_in[2];
  const u16* Cre = (const u16*)d_in[3];
  const u16* Cim = (const u16*)d_in[4];
  const u16* logdt = (const u16*)d_in[5];
  const u16* Dsk = (const u16*)d_in[6];
  const u16* w1 = (const u16*)d_in[8];
  const u16* w2 = (const u16*)d_in[9];
  const u16* gw = (const u16*)d_in[10];
  u16* out = (u16*)d_out;
  char* ws = (char*)d_ws;
  u16* h = (u16*)ws;
  u16* aout = (u16*)(ws + (8u << 20));
  float* g = (float*)(ws + (12u << 20));
  u16* w2p = (u16*)(ws + (16u << 20));

  k1<<<CONV_BLOCKS + G1_BLOCKS + SIDE_BLOCKS, NTHR, 0, stream>>>(u, logAre, Aim, Cre, Cim, logdt, Dsk,
                                                                 w1, gw, h, aout, g, w2, w2p);
  k2<<<G2_TILES_M * G2_TILES_N, 256, 0, stream>>>(h, w2p, aout, g, out);
}
```
